```python
import torch

def _softmax(x, axis=-1):
    return torch.softmax(x, dim=axis)
def _gelu(x):
    return torch.nn.functional.gelu(x, approximate="tanh")
def _silu(x):
    return torch.nn.functional.silu(x)
def _sigmoid(x):
    return torch.sigmoid(x)
def _layernorm(x, axis=-1, eps=1e-5):
    m = x.mean(dim=axis, keepdim=True)
    v = x.var(dim=axis, keepdim=True, unbiased=False)
    return (x - m) / torch.sqrt(v + eps)
def _scatter_add(tgt, idx, src, axis=0):
    return tgt.clone().index_add_(axis, idx, src)
def _segment_sum(src, seg, n):
    out = torch.zeros((n,) + src.shape[1:], dtype=src.dtype)
    return out.index_add_(0, seg, src)
def _pad(x, pw):
    flat = tuple(v for pair in reversed(pw) for v in pair)
    return torch.nn.functional.pad(x, flat)

kernel_name = 'gated_mix__ssm_conv_s4d__swiglu_ffn__prompt'

def setup_inputs():

    _w = (lambda a: (lambda t: t.to(torch.bfloat16) if t.is_floating_point() else t)(torch.as_tensor(a)))
    def _A_setup():
        Bsz, L, D, N = 1, 2048, 640, 192
        return {
            "u": _w((lambda *s: torch.randn(*s, dtype=torch.bfloat16) * 0.05)(Bsz, L, D)),
            "log_A_real": _w((lambda *s: torch.randn(*s, dtype=torch.bfloat16) * 6.4)(D, N) * 0.5),
            "A_imag": _w((lambda *s: torch.randn(*s, dtype=torch.bfloat16) * 0.05)(D, N) * 3.14),
            "C_real": _w((lambda *s: torch.randn(*s, dtype=torch.bfloat16) * 0.05)(D, N) / N ** 0.5),
            "C_imag": _w((lambda *s: torch.randn(*s, dtype=torch.bfloat16) * 0.05)(D, N) / N ** 0.5),
            "log_dt": _w((lambda *s: torch.randn(*s, dtype=torch.bfloat16) * 0.05)(D) * 0.5 - 4.0),
            "D_skip": _w((lambda *s: torch.randn(*s, dtype=torch.bfloat16) * 0.05)(D)),
        }
    _A_in = _A_setup()

    def _B_setup():
        B, S, D, F = 1, 2048, 640, 1536
        return {
            "x": _w((lambda *s: torch.randn(*s, dtype=torch.bfloat16) * 0.05)(B, S, D)),
            "w_gate": _w((lambda *s: torch.randn(*s, dtype=torch.bfloat16) * 0.05)(D, F)),
            "w_up": _w((lambda *s: torch.randn(*s, dtype=torch.bfloat16) * 0.05)(D, F)),
            "w_down": _w((lambda *s: torch.randn(*s, dtype=torch.bfloat16) * 0.05)(F, D)),
        }
    _B_in = _B_setup()

    return {
        "A_u": _A_in["u"],
        "A_log_A_real": _A_in["log_A_real"],
        "A_A_imag": _A_in["A_imag"],
        "A_C_real": _A_in["C_real"],
        "A_C_imag": _A_in["C_imag"],
        "A_log_dt": _A_in["log_dt"],
        "A_D_skip": _A_in["D_skip"],
        "B_x": _B_in["x"],
        "B_w_gate": _B_in["w_gate"],
        "B_w_up": _B_in["w_up"],
        "B_w_down": _B_in["w_down"],
        "gate_w": _w((lambda *s: torch.randn(*s, dtype=torch.bfloat16) * 0.05)(640) * 16.0),
    }

def reference(A_u, A_log_A_real, A_A_imag, A_C_real, A_C_imag, A_log_dt, A_D_skip, B_x, B_w_gate, B_w_up, B_w_down, gate_w):
    def _A_ref(u, log_A_real, A_imag, C_real, C_imag, log_dt, D_skip):
        L = u.shape[1]
        dt = torch.exp(log_dt)
        A = -torch.exp(log_A_real) + 1j * A_imag
        C = (C_real + 1j * C_imag) * dt[:, None]
        pos = (lambda x, dt: x.to(dt))(torch.arange(L), log_dt.dtype)
        P = torch.exp((A * dt[:, None])[:, :, None] * pos[None, None, :])
        K = 2.0 * torch.einsum("dn,dnl->dl", C, P).real
        L2 = 2 * L
        u_t = (lambda x, *d: x.permute(*d))(u, 0, 2, 1)
        Kf = (lambda x, *a, **k: torch.fft.rfft(x.float(), *a, **k))(K, L2)
        Uf = (lambda x, *a, **k: torch.fft.rfft(x.float(), *a, **k))(u_t, L2)
        y = (lambda x, *a, **k: torch.fft.irfft(x, *a, **k).to(torch.bfloat16))(Uf * Kf[None, :, :], L2)[:, :, :L]
        y = (lambda x, *d: x.permute(*d))(y, 0, 2, 1)
        return y + u * D_skip[None, None, :]

    def _B_ref(x, w_gate, w_up, w_down):
        g = _silu(x @ w_gate)
        u = x @ w_up
        return (g * u) @ w_down

    x = A_u
    g = _sigmoid((x * gate_w).sum(-1, keepdims=True))
    a = _A_ref(x, A_log_A_real, A_A_imag, A_C_real, A_C_imag, A_log_dt, A_D_skip)
    b = _B_ref(x, B_w_gate, B_w_up, B_w_down)
    return g * a + (1 - g) * b
```

```cpp
#include <hip/hip_runtime.h>
#include <hip/hip_bf16.h>
#include <stdint.h>

#define LAS __attribute__((address_space(3)))
#define GAS __attribute__((address_space(1)))
#define DEVI __device__ __forceinline__

typedef unsigned short u16;
typedef short s16x8 __attribute__((ext_vector_type(8)));
typedef short s16x4 __attribute__((ext_vector_type(4)));
typedef __bf16 b16x8 __attribute__((ext_vector_type(8)));
typedef __bf16 b16x2 __attribute__((ext_vector_type(2)));
typedef float f32x4 __attribute__((ext_vector_type(4)));
typedef float f32x2 __attribute__((ext_vector_type(2)));
typedef unsigned u32x4 __attribute__((ext_vector_type(4)));
typedef unsigned u32x2 __attribute__((ext_vector_type(2)));
typedef __amdgpu_buffer_rsrc_t rsrc_t;

constexpr int SL = 2048, SD = 640, SN = 192, SF = 1536;
constexpr unsigned OFF_HF = 0, SZ_HF = SL * SF * 2;
constexpr unsigned OFF_WDF = OFF_HF + SZ_HF, SZ_WDF = SF * SD * 2;
constexpr unsigned OFF_UT = OFF_WDF + SZ_WDF, SZ_UT = SL * SD * 2;
constexpr unsigned KRL = 2112;
constexpr unsigned OFF_KR = OFF_UT + SZ_UT, SZ_KR = SD * KRL * 2;
constexpr unsigned OFF_AT = OFF_KR + SZ_KR, SZ_AT = SL * SD * 2;
constexpr unsigned OFF_GATE = OFF_AT + SZ_AT, SZ_GATE = SL * 4;
constexpr unsigned OFF_FLAG = OFF_GATE + SZ_GATE, SZ_FLAG = 8 * 256;
constexpr unsigned WS_USED = OFF_FLAG + SZ_FLAG;
constexpr unsigned OFF_STAMP = 32u << 20;

#define STAMP(k, j) do {} while (0)

DEVI rsrc_t mk_rsrc(const void* p, unsigned bytes) {
  return __builtin_amdgcn_make_buffer_rsrc((void*)p, 0, bytes, 0x00020000);
}
DEVI f32x4 mfma16(s16x8 a, s16x8 b, f32x4 c) {
  return __builtin_amdgcn_mfma_f32_16x16x32_bf16(__builtin_bit_cast(b16x8, a), __builtin_bit_cast(b16x8, b), c, 0, 0, 0);
}
DEVI void dma16(rsrc_t r, LAS char* dst, unsigned voff, unsigned soff) {
  __builtin_amdgcn_raw_ptr_buffer_load_lds(r, (LAS void*)dst, 16, voff, soff, 0, 0);
}
DEVI u32x4 bload16(rsrc_t r, unsigned voff, unsigned soff) {
  return __builtin_bit_cast(u32x4, __builtin_amdgcn_raw_buffer_load_b128(r, voff, soff, 0));
}
DEVI unsigned pk_bf16(float lo, float hi) {
  b16x2 v = {(__bf16)lo, (__bf16)hi};
  return __builtin_bit_cast(unsigned, v);
}
DEVI float bf_lo(unsigned v) { return __builtin_bit_cast(float, v << 16); }
DEVI float bf_hi(unsigned v) { return __builtin_bit_cast(float, v & 0xffff0000u); }
DEVI float bf2f(u16 v) { return __builtin_bit_cast(float, (unsigned)v << 16); }
DEVI float rnd_bf(float x) { return bf_lo(pk_bf16(x, x) & 0xffffu); }
DEVI s16x8 lds_b128(LAS char* p) { return *(LAS s16x8*)p; }
DEVI s16x4 lds_tr(LAS char* p) { return __builtin_amdgcn_ds_read_tr16_b64_v4i16((LAS s16x4*)p); }
DEVI s16x8 cat4(s16x4 a, s16x4 b) { return __builtin_shufflevector(a, b, 0, 1, 2, 3, 4, 5, 6, 7); }
#define WAIT_VM(n) asm volatile("s_waitcnt vmcnt(" #n ")" ::: "memory")
#define WAIT_LGKM0() asm volatile("s_waitcnt lgkmcnt(0)" ::: "memory")
#define BARRIER() do { asm volatile("" ::: "memory"); __builtin_amdgcn_s_barrier(); asm volatile("" ::: "memory"); } while (0)
#define SCHED_FENCE() __builtin_amdgcn_sched_barrier(0)

constexpr int G1_STAGE = 20480;

struct G1Frags { s16x8 x[4]; s16x4 wl[2][3], wh[2][3]; };

#define ASM_TR(dst, addr, off) asm volatile("ds_read_b64_tr_b16 %0, %1 offset:%2" : "=v"(dst) : "v"(addr), "i"(off))
#define ASM_B128(dst, addr, off) asm volatile("ds_read_b128 %0, %1 offset:%2" : "=v"(dst) : "v"(addr), "i"(off))

DEVI void g1_read(G1Frags& f, unsigned sb, unsigned xa, const unsigned (&wa)[3]) {
#pragma unroll
  for (int cc = 0; cc < 3; ++cc) {
    const unsigned a = sb + wa[cc];
    ASM_TR(f.wl[0][cc], a, 0); ASM_TR(f.wh[0][cc], a, 768);
    ASM_TR(f.wl[1][cc], a, 6144); ASM_TR(f.wh[1][cc], a, 6144 + 768);
  }
  const unsigned a = sb + xa;
  ASM_B128(f.x[0], a, 0); ASM_B128(f.x[1], a, 1024); ASM_B128(f.x[2], a, 2048); ASM_B128(f.x[3], a, 3072);
}
DEVI void g1_wait(G1Frags& f) {
  asm volatile("s_waitcnt lgkmcnt(0)"
               : "+v"(f.x[0]), "+v"(f.x[1]), "+v"(f.x[2]), "+v"(f.x[3]),
                 "+v"(f.wl[0][0]), "+v"(f.wl[0][1]), "+v"(f.wl[0][2]), "+v"(f.wl[1][0]), "+v"(f.wl[1][1]), "+v"(f.wl[1][2]),
                 "+v"(f.wh[0][0]), "+v"(f.wh[0][1]), "+v"(f.wh[0][2]), "+v"(f.wh[1][0]), "+v"(f.wh[1][1]), "+v"(f.wh[1][2])
               :: "memory");
}
DEVI void g1_mma(f32x4 (&acc)[2][3][4], const G1Frags& f) {
#pragma unroll
  for (int cc = 0; cc < 3; ++cc)
#pragma unroll
    for (int m = 0; m < 2; ++m) {
      const s16x8 wv = cat4(f.wl[m][cc], f.wh[m][cc]);
#pragma unroll
      for (int tt = 0; tt < 4; ++tt) acc[m][cc][tt] = mfma16(wv, f.x[tt], acc[m][cc][tt]);
    }
}

DEVI void gemm1_role(const u16* x, const u16* wg, const u16* wu, char* ws, LAS char* sm, int b) {
  const int tid = threadIdx.x, lane = tid & 63;
  const int w = __builtin_amdgcn_readfirstlane(tid >> 6);
  STAMP(0, 0);
  const int xcd = b & 7, li = b >> 3;
  const int panel = 8 * (xcd & 1) + (li & 7), col = 4 * (xcd >> 1) + (li >> 3);
  const int row0 = panel * 128, n0 = col * 96;
  const int wm = w >> 1, wn = w & 1, g = lane >> 4, l15 = lane & 15;
  const rsrc_t rx = mk_rsrc(x, SL * SD * 2);
  const rsrc_t rw = mk_rsrc(w < 2 ? wg : wu, SD * SF * 2);

  unsigned xo[2], wo[3];
#pragma unroll
  for (int i = 0; i < 2; ++i) {
    const int row = 16 * (2 * w + i) + (lane >> 2), ch = (lane & 3) ^ ((4 - g) & 3);
    xo[i] = (row0 + row) * (SD * 2) + ch * 16;
  }
#pragma unroll
  for (int i = 0; i < 3; ++i) {
    const int pp = (3 * w + i) % 6, o = pp * 1024 + lane * 16, k = o / 192, chp = (o - 192 * k) >> 4;
    const int n = (((chp >> 1) ^ ((k >> 3) & 1)) << 4) + (chp & 1) * 8;
    wo[i] = (k * SF + n0 + n) * 2;
  }
  auto stage = [&](int s) {
    LAS char* sb = sm + (s & 3) * G1_STAGE;
#pragma unroll
    for (int i = 0; i < 2; ++i) dma16(rx, sb + (2 * w + i) * 1024, xo[i], s * 64);
#pragma unroll
    for (int i = 0; i < 3; ++i) dma16(rw, sb + 8192 + (3 * w + i) * 1024, wo[i], s * (32 * SF * 2));
  };
  const unsigned xa = (64 * wm + l15) * 64 + ((g ^ ((4 - (l15 >> 2)) & 3)) << 4);
  unsigned wa[3];
#pragma unroll
  for (int cc = 0; cc < 3; ++cc)
    wa[cc] = 8192 + (8 * g + (l15 >> 2)) * 192 + (((3 * wn + cc) ^ (g & 1)) << 5) + (lane & 3) * 8;

  f32x4 acc[2][3][4];
#pragma unroll
  for (int m = 0; m < 2; ++m)
#pragma unroll
    for (int cc = 0; cc < 3; ++cc)
#pragma unroll
      for (int tt = 0; tt < 4; ++tt) acc[m][cc][tt] = f32x4{0.f, 0.f, 0.f, 0.f};

  stage(0); stage(1); stage(2); stage(3);
  G1Frags fa, fb;
  WAIT_VM(15); BARRIER();
  STAMP(0, 1);
  const unsigned sbase = (unsigned)(uintptr_t)sm;
  g1_read(fa, sbase, xa, wa);

#define G1_STEP(S, CUR, NXT, VM)                                      \
  do {                                                                \
    g1_wait(CUR);                                                     \
    WAIT_VM(VM);                                                      \
    BARRIER();                                                        \
    if ((S) + 4 < 20) stage((S) + 4);                                 \
    if ((S) + 1 < 20) g1_read(NXT, sbase + (((S) + 1) & 3) * G1_STAGE, xa, wa); \
    SCHED_FENCE();                                                    \
    g1_mma(acc, CUR);                                                 \
    SCHED_FENCE();                                                    \
  } while (0)

  for (int s = 0; s < 16; s += 2) {
    G1_STEP(s, fa, fb, 10);
    G1_STEP(s + 1, fb, fa, 10);
  }
  G1_STEP(16, fa, fb, 10);
  G1_STEP(17, fb, fa, 5);
  G1_STEP(18, fa, fb, 0);
  G1_STEP(19, fb, fa, 0);
#undef G1_STEP
  STAMP(0, 2);

  const rsrc_t rh = mk_rsrc(ws + OFF_HF, SZ_HF);
  const int T0 = (row0 + 64 * wm) >> 4, F0 = 3 * col;
#pragma unroll
  for (int tt = 0; tt < 4; ++tt) {
    u32x2 hv[3];
#pragma unroll
    for (int cc = 0; cc < 3; ++cc) {
      float r[4];
#pragma unroll
      for (int i = 0; i < 4; ++i) {
        const float gv = acc[0][cc][tt][i], uv = acc[1][cc][tt][i];
        const float e = __builtin_amdgcn_exp2f(gv * -1.4426950408889634f);
        r[i] = gv * uv * __builtin_amdgcn_rcpf(1.0f + e);
      }
      hv[cc] = u32x2{pk_bf16(r[0], r[1]), pk_bf16(r[2], r[3])};
    }
    const unsigned base = ((T0 + tt) * 48 + F0) * 1024 + lane * 16;
    if (wn == 0) {
      __builtin_amdgcn_raw_buffer_store_b128(u32x4{hv[0].x, hv[0].y, hv[1].x, hv[1].y}, rh, base, 0, 0);
      __builtin_amdgcn_raw_buffer_store_b64(hv[2], rh, base + 1024, 0, 0);
    } else {
      __builtin_amdgcn_raw_buffer_store_b64(hv[0], rh, base + 1024 + 8, 0, 0);
      __builtin_amdgcn_raw_buffer_store_b128(u32x4{hv[1].x, hv[1].y, hv[2].x, hv[2].y}, rh, base + 2048, 0, 0);
    }
  }
  STAMP(0, 3);
}

DEVI f32x2 cmul(f32x2 a, f32x2 b) {
  const f32x2 t = f32x2{a.x, a.x} * b;
  return __builtin_elementwise_fma(f32x2{a.y, a.y}, f32x2{-b.y, b.x}, t);
}
DEVI f32x2 cexp_pos(float zr2, float zi2, float p) {
  const float e = __builtin_amdgcn_exp2f(zr2 * p);
  const float th = __builtin_amdgcn_fractf(zi2 * p);
  return f32x2{e * __builtin_amdgcn_cosf(th), e * __builtin_amdgcn_sinf(th)};
}
constexpr int KG_ROW = 288;
constexpr int KG_LDS = 57 * KG_ROW;

DEVI void kgen_wave(const u16* logA, const u16* Aim, const u16* Cre, const u16* Cim, const u16* logdt,
                    char* ws, LAS char* sm, int d, int lane) {
  const int g = lane >> 4, l15 = lane & 15;
  const float dt = rnd_bf(expf(bf2f(logdt[d])));
  f32x4 acc[3] = {f32x4{0.f, 0.f, 0.f, 0.f}, f32x4{0.f, 0.f, 0.f, 0.f}, f32x4{0.f, 0.f, 0.f, 0.f}};
  u16 pla[3], pai[3], pcr[3], pci[3];
#pragma unroll
  for (int c = 0; c < 3; ++c) {
    const int idx = d * SN + 64 * c + lane;
    pla[c] = logA[idx]; pai[c] = Aim[idx]; pcr[c] = Cre[idx]; pci[c] = Cim[idx];
  }
  LAS char* tw = sm + lane * 4;
  auto putx = [&](int row, f32x2 v) { *(LAS unsigned*)(tw + row * KG_ROW) = pk_bf16(v.x, -v.y); };
  auto puty = [&](int row, f32x2 v) { *(LAS unsigned*)(tw + (41 + row) * KG_ROW) = pk_bf16(v.x, v.y); };
#pragma unroll 1
  for (int c = 0; c < 3; ++c) {
    const u16 la = c == 0 ? pla[0] : c == 1 ? pla[1] : pla[2];
    const u16 ai = c == 0 ? pai[0] : c == 1 ? pai[1] : pai[2];
    const u16 cr = c == 0 ? pcr[0] : c == 1 ? pcr[1] : pcr[2];
    const u16 ci = c == 0 ? pci[0] : c == 1 ? pci[1] : pci[2];
    const float zr = -rnd_bf(expf(bf2f(la))) * dt, zi = bf2f(ai) * dt;
    const float zr2 = zr * 1.4426950408889634f, zi2 = zi * 0.15915494309189535f;
    const f32x2 C = {bf2f(cr) * dt, bf2f(ci) * dt};
    const f32x2 w1 = cexp_pos(zr2, zi2, 1.f), w128 = cexp_pos(zr2, zi2, 128.f);
    const f32x2 w2 = cmul(w1, w1), w4 = cmul(w2, w2), w8 = cmul(w4, w4);
    const f32x2 w256 = cmul(w128, w128), w512 = cmul(w256, w256), w1024 = cmul(w512, w512);
    {
      f32x2 y = {1.f, 0.f};
      puty(0, y);
      y = w8; puty(1, y);
#pragma unroll
      for (int bb = 2; bb < 16; ++bb) { y = cmul(y, w8); puty(bb, y); }
    }
    {
      f32x2 v = C;
#pragma unroll
      for (int k = 0; k < 8; ++k) {
        if (k) v = cmul(v, w1);
        putx(k, v); putx(8 + k, cmul(v, w128));
      }
    }
    {
      f32x2 v = cmul(C, w256);
#pragma unroll
      for (int k = 0; k < 4; ++k) {
        if (k) v = cmul(v, w2);
        putx(16 + k, v); putx(20 + k, cmul(v, w128));
      }
    }
    {
      f32x2 v0 = cmul(C, w512), v1 = cmul(v0, w4);
#pragma unroll
      for (int k = 0; k < 4; ++k) {
        if (k) { v0 = cmul(v0, w128); v1 = cmul(v1, w128); }
        putx(24 + 2 * k, v0); putx(25 + 2 * k, v1);
      }
    }
    {
      f32x2 v = cmul(C, w1024);
#pragma unroll
      for (int k = 0; k < 9; ++k) {
        if (k) v = cmul(v, w128);
        putx(32 + k, v);
      }
    }
    WAIT_LGKM0();
#pragma unroll
    for (int s = 0; s < 4; ++s) {
      const unsigned o = l15 * KG_ROW + 64 * s + 16 * g;
      const s16x8 yb = lds_b128(sm + 41 * KG_ROW + o);
#pragma unroll
      for (int i = 0; i < 3; ++i) acc[i] = mfma16(lds_b128(sm + i * 16 * KG_ROW + o), yb, acc[i]);
    }
    WAIT_LGKM0();
  }
  LAS float* kq = (LAS float*)sm;
#pragma unroll
  for (int i = 0; i < 4; ++i) {
    const int r = 4 * g + i;
    kq[(r & 7) + 128 * (r >> 3) + 8 * l15] = 2.f * acc[0][i];
    const int rr = r - 8;
    const int i1 = r < 8 ? 256 + (r & 3) + 64 * (r >> 2) + 4 * l15 : 384 + (rr & 1) + 32 * (rr >> 1) + 2 * l15;
    kq[i1] = 2.f * acc[1][i];
    if (r < 8) kq[512 + 16 * r + l15] = 2.f * acc[2][i];
    else if (r == 8 && l15 == 0) kq[640] = 2.f * acc[2][i];
  }
  WAIT_LGKM0();
  const rsrc_t rk = mk_rsrc(ws + OFF_KR, SZ_KR);
#pragma unroll
  for (int k = 0; k < 4; ++k) {
    const int i0 = 8 * (64 * k + lane);
    unsigned o[4];
#pragma unroll
    for (int j = 0; j < 8; j += 2) {
      const int la = 2047 - i0 - j, lb = la - 1;
      const unsigned bits = pk_bf16((float)la, (float)lb);
      const int ia = la < 256 ? la : (int)(bits & 0xffffu) - 17024;
      const int ib = lb < 256 ? lb : (int)(bits >> 16) - 17024;
      o[j >> 1] = pk_bf16(kq[ia], kq[ib]);
    }
    __builtin_amdgcn_raw_buffer_store_b128(u32x4{o[0], o[1], o[2], o[3]}, rk, d * (KRL * 2) + i0 * 2, 0, 0);
  }
  if (lane < 8) __builtin_amdgcn_raw_buffer_store_b128(u32x4{0, 0, 0, 0}, rk, d * (KRL * 2) + 4096 + lane * 16, 0, 0);
}

DEVI void misc_wave(const u16* u, const u16* wd, const u16* gw, char* ws, LAS char* sm, int i, int lane) {
  const int g = lane >> 4, l15 = lane & 15, q = l15 >> 2, p = lane & 3;
  const rsrc_t ru = mk_rsrc(u, SL * SD * 2);
  const rsrc_t rwd = mk_rsrc(wd, SF * SD * 2);
  const int rt = i >> 3, cg = i & 7;
#pragma unroll
  for (int k = 0; k < 10; ++k) {
    const int c = 64 * k + lane, row = c / 10, ch = c - 10 * row;
    dma16(ru, sm + k * 1024, ((64 * rt + row) * SD + 80 * cg) * 2 + ch * 16, 0);
  }
  const int Fb = i / 5, Dg = i - 5 * Fb;
  if (i < 240) {
#pragma unroll
    for (int k = 0; k < 8; ++k) {
      const int row = 4 * k + (lane >> 4);
      dma16(rwd, sm + 10240 + k * 1024, ((32 * Fb + row) * SD + 128 * Dg) * 2 + l15 * 16, 0);
    }
  }
  {
    const int row = 8 * i + (lane >> 3), seg = lane & 7;
    const rsrc_t rg = mk_rsrc(gw, SD * 2);
    float s = 0.f;
#pragma unroll
    for (int k = 0; k < 10; ++k) {
      const u32x4 xv = bload16(ru, row * (SD * 2) + (seg + 8 * k) * 16, 0);
      const u32x4 gv = bload16(rg, (seg + 8 * k) * 16, 0);
#pragma unroll
      for (int j = 0; j < 4; ++j) {
        s = fmaf(bf_lo(xv[j]), bf_lo(gv[j]), s);
        s = fmaf(bf_hi(xv[j]), bf_hi(gv[j]), s);
      }
    }
    s += __shfl_xor(s, 1); s += __shfl_xor(s, 2); s += __shfl_xor(s, 4);
    s = rnd_bf(s);
    const float gt = rnd_bf(1.0f / (1.0f + expf(-s)));
    if (seg == 0) ((float*)(ws + OFF_GATE))[row] = gt;
  }
  if (i == 0 && lane < 8) ((unsigned*)(ws + OFF_FLAG))[lane * 64] = 0u;
  WAIT_VM(0);
  const rsrc_t rut = mk_rsrc(ws + OFF_UT, SZ_UT);
#pragma unroll
  for (int c = 0; c < 5; ++c)
#pragma unroll
    for (int h = 0; h < 2; ++h) {
      const int t0 = 32 * h + 8 * g;
      LAS char* a = sm + (t0 + q) * 160 + c * 32 + p * 8;
      const s16x8 v = cat4(lds_tr(a), lds_tr(a + 4 * 160));
      __builtin_amdgcn_raw_buffer_store_b128(__builtin_bit_cast(u32x4, v), rut,
                                             ((80 * cg + 16 * c + l15) * SL + 64 * rt + t0) * 2, 0, 0);
    }
  if (i < 240) {
    const rsrc_t rf = mk_rsrc(ws + OFF_WDF, SZ_WDF);
#pragma unroll
    for (int c = 0; c < 8; ++c) {
      LAS char* a = sm + 10240 + (4 * g + q) * 256 + c * 32 + p * 8;
      const s16x8 v = cat4(lds_tr(a), lds_tr(a + 16 * 256));
      __builtin_amdgcn_raw_buffer_store_b128(__builtin_bit_cast(u32x4, v), rf,
                                             ((8 * Dg + c) * 48 + Fb) * 1024 + lane * 16, 0, 0);
    }
  }
}

constexpr int K1_LDS = 81920;

__global__ __launch_bounds__(256, 2) void k1_ffn_in_and_prep(
    const u16* __restrict__ u, const u16* __restrict__ logA, const u16* __restrict__ Aim,
    const u16* __restrict__ Cre, const u16* __restrict__ Cim, const u16* __restrict__ logdt,
    const u16* __restrict__ wg, const u16* __restrict__ wu, const u16* __restrict__ wd,
    const u16* __restrict__ gw, char* __restrict__ ws) {
  extern __shared__ __attribute__((aligned(16))) char smem[];
  LAS char* sm = (LAS char*)smem;
  const int b = blockIdx.x;
  if (b < 256) {
    gemm1_role(u, wg, wu, ws, sm, b);
  } else {
    const int i = b - 256, lane = threadIdx.x & 63;
    const int w = __builtin_amdgcn_readfirstlane(threadIdx.x >> 6);
    if (w < 3) {
      const int d = i + 256 * w;
      if (d < SD)
        kgen_wave(logA, Aim, Cre, Cim, logdt, ws, sm + w * KG_LDS, d, lane);
    } else {
      misc_wave(u, wd, gw, ws, sm + 3 * KG_LDS, i, lane);
    }
  }
}

constexpr int CP_STRIDE = 4416;
constexpr int CP_CH = 4 * CP_STRIDE;
constexpr int U_BLK = 160, U_CH = 47 * U_BLK;
constexpr int L2_CP = 0;
constexpr int L2_U = L2_CP + 3 * CP_CH;
constexpr int L2_ST = L2_U + 3 * U_CH;
constexpr int L2_RED = L2_ST + 4 * 2048;
constexpr int L2_SYNC = L2_RED + 61440;
constexpr int L2_LDS = L2_SYNC + 64;
constexpr int L2_AT = 0;
constexpr int L2_OUT = 10240;

DEVI s16x8 toep_frag(LAS char* p) {
  const u32x2 a = *(volatile LAS u32x2*)p, b = *(volatile LAS u32x2*)(p + 8);
  return __builtin_bit_cast(s16x8, u32x4{a.x, a.y, b.x, b.y});
}

DEVI void conv_build(char* ws, LAS char* cp, LAS char* ub, int d, int lane) {
  const rsrc_t rk = mk_rsrc(ws + OFF_KR, SZ_KR);
  const rsrc_t rut = mk_rsrc(ws + OFF_UT, SZ_UT);
  u32x4 kv[5]; u32x2 kn[5]; u32x4 uv[4];
#pragma unroll
  for (int k = 0; k < 5; ++k) {
    const int m = 64 * k + lane;
    const unsigned o = (m < 264) ? d * (KRL * 2) + m * 16 : 0xfffffff0u;
    kv[k] = bload16(rk, o, 0);
    const unsigned o2 = (m < 263) ? o + 16 : 0xfffffff0u;
    kn[k] = __builtin_bit_cast(u32x2, __builtin_amdgcn_raw_buffer_load_b64(rk, o2, 0, 0));
  }
#pragma unroll
  for (int k = 0; k < 4; ++k) uv[k] = bload16(rut, d * (SL * 2) + (64 * k + lane) * 16, 0);
#pragma unroll
  for (int k = 0; k < 3; ++k) {
    const int c = 64 * k + lane;
    if (c < 150) *(LAS u32x4*)(ub + c * 16) = u32x4{0, 0, 0, 0};
  }
#pragma unroll
  for (int k = 0; k < 5; ++k) {
    const int m = 64 * k + lane;
    if (m < 264) {
      const u32x4 a = kv[k]; const u32x2 n = kn[k];
      *(LAS u32x4*)(cp + m * 16) = a;
      *(LAS u32x4*)(cp + CP_STRIDE + m * 16) =
          u32x4{__builtin_amdgcn_alignbyte(a.y, a.x, 2), __builtin_amdgcn_alignbyte(a.z, a.y, 2),
                __builtin_amdgcn_alignbyte(a.w, a.z, 2), __builtin_amdgcn_alignbyte(n.x, a.w, 2)};
      *(LAS u32x4*)(cp + 2 * CP_STRIDE + m * 16) = u32x4{a.y, a.z, a.w, n.x};
      *(LAS u32x4*)(cp + 3 * CP_STRIDE + m * 16) =
          u32x4{__builtin_amdgcn_alignbyte(a.z, a.y, 2), __builtin_amdgcn_alignbyte(a.w, a.z, 2),
                __builtin_amdgcn_alignbyte(n.x, a.w, 2), __builtin_amdgcn_alignbyte(n.y, n.x, 2)};
    }
  }
#pragma unroll
  for (int k = 0; k < 4; ++k) {
    const int m = 64 * k + lane;
    *(LAS u32x4*)(ub + (15 + (m >> 3)) * U_BLK + (m & 7) * 16) = uv[k];
  }
}

template <int NMT>
DEVI void conv_store(const f32x4 (&acc)[NMT], int mt0, int j, int d, float dsk, char* ws, LAS char* ub, LAS char* st, int lane) {
  const int g = lane >> 4, l15 = lane & 15;
#pragma unroll
  for (int m = 0; m < NMT; ++m)
    *(LAS u32x2*)(st + l15 * 128 + (mt0 + m) * 32 + g * 8) =
        u32x2{pk_bf16(acc[m][0], acc[m][1]), pk_bf16(acc[m][2], acc[m][3])};
  WAIT_LGKM0();
  const rsrc_t ra = mk_rsrc(ws + OFF_AT, SZ_AT);
  constexpr int PER = NMT * 2;
#pragma unroll
  for (int k = 0; k < 16 * PER / 64; ++k) {
    const int c = 64 * k + lane, blk = c / PER, pc = 2 * mt0 + c % PER;
    const u32x4 yv = *(LAS u32x4*)(st + blk * 128 + pc * 16);
    const u32x4 uv = *(LAS u32x4*)(ub + (15 + 16 * j + blk) * U_BLK + pc * 16);
    u32x4 o;
#pragma unroll
    for (int e = 0; e < 4; ++e) {
      const unsigned sk = pk_bf16(bf_lo(uv[e]) * dsk, bf_hi(uv[e]) * dsk);
      o[e] = pk_bf16(bf_lo(yv[e]) + bf_lo(sk), bf_hi(yv[e]) + bf_hi(sk));
    }
    __builtin_amdgcn_raw_buffer_store_b128(o, ra, ((16 * j + blk) * SD + d) * 128 + pc * 16, 0, 16);
  }
  WAIT_LGKM0();
}

DEVI void conv_unit4(int j, int d, float dsk, char* ws, LAS char* cp, LAS char* ub, LAS char* st, int lane) {
  const int g = lane >> 4, r = lane & 15;
  const int i0 = 2047 - r + 8 * g, c = i0 & 3;
  LAS char* ap = cp + c * CP_STRIDE + (i0 - c) * 2;
  LAS char* bp = ub + (15 + 16 * j + r) * U_BLK + 16 * g;
  f32x4 acc[4] = {f32x4{0, 0, 0, 0}, f32x4{0, 0, 0, 0}, f32x4{0, 0, 0, 0}, f32x4{0, 0, 0, 0}};
  s16x8 am2 = toep_frag(ap + 64), am1 = toep_frag(ap + 32);
  const int nd = 16 * (j + 1);
#pragma unroll 2
  for (int dl = 0; dl < nd; ++dl) {
    LAS char* a = ap - 128 * dl;
    LAS char* bq = bp - U_BLK * dl;
    const s16x8 b0 = lds_b128(bq), b1 = lds_b128(bq + 64);
    const s16x8 a0 = toep_frag(a), a1 = toep_frag(a - 32), a2 = toep_frag(a - 64), a3 = toep_frag(a - 96);
    acc[0] = mfma16(am2, b1, acc[0]);
    acc[1] = mfma16(am1, b1, acc[1]);
    acc[0] = mfma16(a0, b0, acc[0]);
    acc[1] = mfma16(a1, b0, acc[1]);
    acc[2] = mfma16(a0, b1, acc[2]);
    acc[3] = mfma16(a1, b1, acc[3]);
    acc[2] = mfma16(a2, b0, acc[2]);
    acc[3] = mfma16(a3, b0, acc[3]);
    am2 = a2; am1 = a3;
  }
  conv_store<4>(acc, 0, j, d, dsk, ws, ub, st, lane);
}

DEVI void conv_unit2(int mt0, int d, float dsk, char* ws, LAS char* cp, LAS char* ub, LAS char* st, int lane) {
  const int g = lane >> 4, r = lane & 15;
  const int i0 = 2047 - r + 8 * g, c = i0 & 3;
  LAS char* ap = cp + c * CP_STRIDE + (i0 - c) * 2 - 32 * mt0;
  LAS char* bp = ub + (15 + r) * U_BLK + 16 * g;
  f32x4 a0c[2] = {f32x4{0, 0, 0, 0}, f32x4{0, 0, 0, 0}}, a1c[2] = {f32x4{0, 0, 0, 0}, f32x4{0, 0, 0, 0}};
#pragma unroll 2
  for (int dl = 0; dl < 32; ++dl) {
    LAS char* a = ap - 128 * dl;
    LAS char* bq = bp - U_BLK * dl;
    const s16x8 x0 = toep_frag(a), x1 = toep_frag(a - 32), z0 = toep_frag(a + 64), z1 = toep_frag(a + 32);
    const s16x8 p0 = lds_b128(bq + 16 * U_BLK), p1 = lds_b128(bq + 16 * U_BLK + 64);
    a1c[0] = mfma16(x0, p0, a1c[0]);
    a1c[1] = mfma16(x1, p0, a1c[1]);
    a1c[0] = mfma16(z0, p1, a1c[0]);
    a1c[1] = mfma16(z1, p1, a1c[1]);
    if (dl < 16) {
      const s16x8 q0 = lds_b128(bq), q1 = lds_b128(bq + 64);
      a0c[0] = mfma16(x0, q0, a0c[0]);
      a0c[1] = mfma16(x1, q0, a0c[1]);
      a0c[0] = mfma16(z0, q1, a0c[0]);
      a0c[1] = mfma16(z1, q1, a0c[1]);
    }
  }
  conv_store<2>(a0c, mt0, 0, d, dsk, ws, ub, st, lane);
  conv_store<2>(a1c, mt0, 1, d, dsk, ws, ub, st, lane);
}

__global__ __launch_bounds__(512, 2) void k2_conv_ffn_out(const u16* __restrict__ dskip, char* __restrict__ ws,
                                                          u16* __restrict__ out) {
  extern __shared__ __attribute__((aligned(16))) char smem[];
  LAS char* sm = (LAS char*)smem;
  const int tid = threadIdx.x, lane = tid & 63;
  const int w = __builtin_amdgcn_readfirstlane(tid >> 6);
  const int b = blockIdx.x, xcd = b & 7, li = b >> 3;
  const int rt = 8 * (xcd & 3) + (li & 7), cg = 4 * (xcd >> 2) + (li >> 3);
  const int g = lane >> 4, l15 = lane & 15;
  LAS unsigned* sync = (LAS unsigned*)(sm + L2_SYNC);
  if (tid < 4) sync[tid] = 0u;
  WAIT_LGKM0();
  BARRIER();

  f32x4 acc[4][5];
  if (w < 4) {
    const rsrc_t rh = mk_rsrc(ws + OFF_HF, SZ_HF);
    const rsrc_t rf = mk_rsrc(ws + OFF_WDF, SZ_WDF);
    const unsigned ho = (4 * rt * 48 + 12 * w) * 1024 + lane * 16;
    const unsigned wo = (5 * cg * 48 + 12 * w) * 1024 + lane * 16;
#pragma unroll
    for (int tt = 0; tt < 4; ++tt)
#pragma unroll
      for (int dd = 0; dd < 5; ++dd) acc[tt][dd] = f32x4{0.f, 0.f, 0.f, 0.f};
    u32x4 hb[3][4], wb[3][5];
    auto load = [&](int s, int slot) {
#pragma unroll
      for (int dd = 0; dd < 5; ++dd) wb[slot][dd] = bload16(rf, wo, dd * 48 * 1024 + s * 1024);
#pragma unroll
      for (int tt = 0; tt < 4; ++tt) hb[slot][tt] = bload16(rh, ho, tt * 48 * 1024 + s * 1024);
    };
    load(0, 0); load(1, 1); load(2, 2);
    SCHED_FENCE();
#pragma unroll
    for (int s = 0; s < 12; ++s) {
      const int slot = s % 3;
#pragma unroll
      for (int tt = 0; tt < 4; ++tt)
#pragma unroll
        for (int dd = 0; dd < 5; ++dd)
          acc[tt][dd] = mfma16(__builtin_bit_cast(s16x8, wb[slot][dd]), __builtin_bit_cast(s16x8, hb[slot][tt]), acc[tt][dd]);
      SCHED_FENCE();
      if (s + 3 < 12) load(s + 3, slot);
      SCHED_FENCE();
    }
    LAS char* red = sm + L2_RED;
#pragma unroll
    for (int tt = 0; tt < 4; ++tt) {
      if (tt != w) {
        const int slot = (tt * 3 + (w > tt ? w - 1 : w)) * 5;
#pragma unroll
        for (int dd = 0; dd < 5; ++dd) *(LAS f32x4*)(red + (slot + dd) * 1024 + lane * 16) = acc[tt][dd];
      }
    }
    WAIT_LGKM0();
  } else {
    __builtin_amdgcn_s_setprio(2);
    const int cw = w - 4, pi = rt >> 1, odd = rt & 1;
    const int dX = 80 * cg + 5 * pi + (odd ? 3 : 0), dY = dX + 1, dZ = 80 * cg + 5 * pi + 2;
    LAS char* st = sm + L2_ST + cw * 2048;
    if (cw != 2) {
      const int slot = cw == 3 ? 2 : cw, d = cw == 0 ? dX : cw == 1 ? dY : dZ;
      LAS char* cp = sm + L2_CP + slot * CP_CH;
      LAS char* ub = sm + L2_U + slot * U_CH;
      const float dsk = bf2f(dskip[d]);
      conv_build(ws, cp, ub, d, lane);
      WAIT_LGKM0();
      if (cw < 2 && lane == 0) __hip_atomic_store(sync + cw, 1u, __ATOMIC_RELAXED, __HIP_MEMORY_SCOPE_WORKGROUP);
      if (cw < 2) conv_unit4(1, d, dsk, ws, cp, ub, st, lane);
      else conv_unit2(odd ? 2 : 0, d, dsk, ws, cp, ub, st, lane);
    } else {
      const float dx = bf2f(dskip[dX]), dy = bf2f(dskip[dY]);
      while (__hip_atomic_load(sync + 0, __ATOMIC_RELAXED, __HIP_MEMORY_SCOPE_WORKGROUP) == 0u) __builtin_amdgcn_s_sleep(1);
      asm volatile("" ::: "memory");
      conv_unit4(0, dX, dx, ws, sm + L2_CP, sm + L2_U, st, lane);
      while (__hip_atomic_load(sync + 1, __ATOMIC_RELAXED, __HIP_MEMORY_SCOPE_WORKGROUP) == 0u) __builtin_amdgcn_s_sleep(1);
      asm volatile("" ::: "memory");
      conv_unit4(0, dY, dy, ws, sm + L2_CP + CP_CH, sm + L2_U + U_CH, st, lane);
    }
    WAIT_VM(0);
    unsigned arrived = 0;
    if (lane == 0) arrived = __hip_atomic_fetch_add(sync + 2, 1u, __ATOMIC_RELAXED, __HIP_MEMORY_SCOPE_WORKGROUP);
    arrived = __builtin_amdgcn_readfirstlane(arrived);
    GAS unsigned* flag = (GAS unsigned*)(ws + OFF_FLAG) + cg * 64;
    if (arrived == 3u && lane == 0) __hip_atomic_fetch_add(flag, 1u, __ATOMIC_RELAXED, __HIP_MEMORY_SCOPE_AGENT);
    if (arrived == 3u) {
      unsigned spins = 0;
      while (__builtin_amdgcn_readfirstlane(__hip_atomic_load(flag, __ATOMIC_RELAXED, __HIP_MEMORY_SCOPE_AGENT)) < 32u) {
        __builtin_amdgcn_s_sleep(1);
        if (++spins > (1u << 20)) break;
      }
      asm volatile("" ::: "memory");
      const rsrc_t ra = mk_rsrc(ws + OFF_AT, SZ_AT);
      u32x4 av[10];
#pragma unroll
      for (int k = 0; k < 10; ++k)
        av[k] = __builtin_bit_cast(u32x4, __builtin_amdgcn_raw_buffer_load_b128(ra, (rt * SD + 80 * cg) * 128 + (64 * k + lane) * 16, 0, 16));
#pragma unroll
      for (int k = 0; k < 10; ++k) *(LAS u32x4*)(sm + L2_AT + (64 * k + lane) * 16) = av[k];
      WAIT_LGKM0();
    }
  }
  BARRIER();
  if (w < 4) {
    LAS char* red = sm + L2_RED;
    f32x4 bs[5];
#pragma unroll
    for (int dd = 0; dd < 5; ++dd) {
      f32x4 v = w == 0 ? acc[0][dd] : w == 1 ? acc[1][dd] : w == 2 ? acc[2][dd] : acc[3][dd];
#pragma unroll
      for (int k = 0; k < 3; ++k) v += *(LAS f32x4*)(red + ((w * 3 + k) * 5 + dd) * 1024 + lane * 16);
      bs[dd] = v;
    }
    const float gt = ((const float*)(ws + OFF_GATE))[64 * rt + 16 * w + l15];
    const float og = rnd_bf(1.0f - gt);
    LAS char* os = sm + L2_OUT + w * 2560;
#pragma unroll
    for (int dd = 0; dd < 5; ++dd) {
      const s16x4 av = lds_tr(sm + L2_AT + (16 * dd + 4 * g + (l15 >> 2)) * 128 + (16 * w + 4 * (lane & 3)) * 2);
      float r[4];
#pragma unroll
      for (int e = 0; e < 4; ++e) {
        const float a = bf2f((u16)av[e]);
        r[e] = rnd_bf(gt * a) + rnd_bf(og * rnd_bf(bs[dd][e]));
      }
      *(LAS u32x2*)(os + l15 * 160 + dd * 32 + g * 8) = u32x2{pk_bf16(r[0], r[1]), pk_bf16(r[2], r[3])};
    }
    WAIT_LGKM0();
    const rsrc_t ro = mk_rsrc(out, SL * SD * 2);
#pragma unroll
    for (int k = 0; k < 3; ++k) {
      const int c = 64 * k + lane;
      if (c < 160) {
        const int row = c / 10, ch = c - 10 * row;
        const u32x4 v = *(LAS u32x4*)(os + c * 16);
        __builtin_amdgcn_raw_buffer_store_b128(v, ro, ((64 * rt + 16 * w + row) * SD + 80 * cg) * 2 + ch * 16, 0, 0);
      }
    }
  }
}

extern "C" void kernel_launch(void* const* d_in, const int* in_sizes, int n_in, void* d_out, int out_size,
                              void* d_ws, size_t ws_size, hipStream_t stream) {
  const u16* u = (const u16*)d_in[0];
  const u16* logA = (const u16*)d_in[1];
  const u16* Aim = (const u16*)d_in[2];
  const u16* Cre = (const u16*)d_in[3];
  const u16* Cim = (const u16*)d_in[4];
  const u16* logdt = (const u16*)d_in[5];
  const u16* dskip = (const u16*)d_in[6];
  const u16* wg = (const u16*)d_in[8];
  const u16* wu = (const u16*)d_in[9];
  const u16* wd = (const u16*)d_in[10];
  const u16* gw = (const u16*)d_in[11];
  char* ws = (char*)d_ws;
  (void)hipFuncSetAttribute((const void*)k1_ffn_in_and_prep, hipFuncAttributeMaxDynamicSharedMemorySize, K1_LDS);
  (void)hipFuncSetAttribute((const void*)k2_conv_ffn_out, hipFuncAttributeMaxDynamicSharedMemorySize, L2_LDS);
  k1_ffn_in_and_prep<<<512, 256, K1_LDS, stream>>>(u, logA, Aim, Cre, Cim, logdt, wg, wu, wd, gw, ws);
  k2_conv_ffn_out<<<256, 512, L2_LDS, stream>>>(dskip, ws, (u16*)d_out);
}
```

```cpp
#include <hip/hip_runtime.h>
#include <hip/hip_bf16.h>
#include <stdint.h>

#define LAS __attribute__((address_space(3)))
#define GAS __attribute__((address_space(1)))
#define DEVI __device__ __forceinline__

typedef unsigned short u16;
typedef unsigned long long u64;
typedef short s16x8 __attribute__((ext_vector_type(8)));
typedef short s16x4 __attribute__((ext_vector_type(4)));
typedef __bf16 b16x8 __attribute__((ext_vector_type(8)));
typedef __bf16 b16x2 __attribute__((ext_vector_type(2)));
typedef float f32x4 __attribute__((ext_vector_type(4)));
typedef float f32x2 __attribute__((ext_vector_type(2)));
typedef unsigned u32x4 __attribute__((ext_vector_type(4)));
typedef unsigned u32x2 __attribute__((ext_vector_type(2)));
typedef __amdgpu_buffer_rsrc_t rsrc_t;
typedef GAS u64 gu64;

constexpr int SL = 2048, SD = 640, SN = 192, SF = 1536;
constexpr unsigned OFF_HF = 0, SZ_HF = SL * SF * 2;
constexpr unsigned OFF_WDF = OFF_HF + SZ_HF, SZ_WDF = SF * SD * 2;
constexpr unsigned OFF_UT = OFF_WDF + SZ_WDF, SZ_UT = SL * SD * 2;
constexpr unsigned OFF_AT = OFF_UT + SZ_UT, SZ_AT = SL * SD * 2;
constexpr unsigned OFF_GATE = OFF_AT + SZ_AT, SZ_GATE = SL * 4;
constexpr unsigned OFF_FLAG = OFF_GATE + SZ_GATE;
constexpr int FL_E = 0, FL_H = 32, FL_M = FL_H + 256, FL_A = FL_M + 256;
constexpr unsigned OFF_STAMP = 32u << 20;
constexpr int LDS_BYTES = 81920;
#define PRIO_KGEN 0
#define PRIO_CONV 0
#define PRIO_G1 0
#define PRIO_G2 0

#define STAMP_CLOCK() __builtin_amdgcn_s_memrealtime()
#define STAMP(k, j) do {} while (0)

DEVI rsrc_t mk_rsrc(const void* p, unsigned bytes) {
  return __builtin_amdgcn_make_buffer_rsrc((void*)p, 0, bytes, 0x00020000);
}
DEVI f32x4 mfma16(s16x8 a, s16x8 b, f32x4 c) {
  return __builtin_amdgcn_mfma_f32_16x16x32_bf16(__builtin_bit_cast(b16x8, a), __builtin_bit_cast(b16x8, b), c, 0, 0, 0);
}
DEVI f32x4 mfma16u(u32x4 a, u32x4 b, f32x4 c) {
  return __builtin_amdgcn_mfma_f32_16x16x32_bf16(__builtin_bit_cast(b16x8, a), __builtin_bit_cast(b16x8, b), c, 0, 0, 0);
}
DEVI void dma16(rsrc_t r, LAS char* dst, unsigned voff, unsigned soff) {
  __builtin_amdgcn_raw_ptr_buffer_load_lds(r, (LAS void*)dst, 16, voff, soff, 0, 0);
}
DEVI u32x4 bload16(rsrc_t r, unsigned voff, unsigned soff) {
  return __builtin_bit_cast(u32x4, __builtin_amdgcn_raw_buffer_load_b128(r, voff, soff, 0));
}
DEVI u32x4 bload16_sc1(rsrc_t r, unsigned voff, unsigned soff) {
  return __builtin_bit_cast(u32x4, __builtin_amdgcn_raw_buffer_load_b128(r, voff, soff, 16));
}
DEVI void bstore16_sc1(u32x4 v, rsrc_t r, unsigned voff) { __builtin_amdgcn_raw_buffer_store_b128(v, r, voff, 0, 16); }
DEVI u64 flag_load(gu64* p) { return __hip_atomic_load(p, __ATOMIC_RELAXED, __HIP_MEMORY_SCOPE_AGENT); }
DEVI void flag_store(gu64* p, u64 v) { __hip_atomic_store(p, v, __ATOMIC_RELAXED, __HIP_MEMORY_SCOPE_AGENT); }
DEVI void flags_wait(gu64* p, bool vote, u64 tag) {
  for (unsigned spins = 0; spins < (1u << 18); ++spins) {
    const u64 v = flag_load(p);
    if (__all(!vote || v == tag)) break;
    __builtin_amdgcn_s_sleep(2);
  }
  asm volatile("" ::: "memory");
}
DEVI void lds_wait(LAS unsigned* p, unsigned want) {
  for (unsigned spins = 0; spins < (1u << 22); ++spins) {
    if (__builtin_amdgcn_readfirstlane(__hip_atomic_load(p, __ATOMIC_RELAXED, __HIP_MEMORY_SCOPE_WORKGROUP)) >= want) break;
    __builtin_amdgcn_s_sleep(1);
  }
  asm volatile("" ::: "memory");
}
DEVI unsigned pk_bf16(float lo, float hi) {
  b16x2 v = {(__bf16)lo, (__bf16)hi};
  return __builtin_bit_cast(unsigned, v);
}
DEVI float bf_lo(unsigned v) { return __builtin_bit_cast(float, v << 16); }
DEVI float bf_hi(unsigned v) { return __builtin_bit_cast(float, v & 0xffff0000u); }
DEVI float bf2f(u16 v) { return __builtin_bit_cast(float, (unsigned)v << 16); }
DEVI float rnd_bf(float x) { return bf_lo(pk_bf16(x, x) & 0xffffu); }
DEVI s16x8 lds_b128(LAS char* p) { return *(LAS s16x8*)p; }
DEVI s16x4 lds_tr(LAS char* p) { return __builtin_amdgcn_ds_read_tr16_b64_v4i16((LAS s16x4*)p); }
DEVI s16x8 cat4(s16x4 a, s16x4 b) { return __builtin_shufflevector(a, b, 0, 1, 2, 3, 4, 5, 6, 7); }
#define WAIT_VM(n) asm volatile("s_waitcnt vmcnt(" #n ")" ::: "memory")
#define WAIT_LGKM0() asm volatile("s_waitcnt lgkmcnt(0)" ::: "memory")
#define BARRIER() do { asm volatile("" ::: "memory"); __builtin_amdgcn_s_barrier(); asm volatile("" ::: "memory"); } while (0)
#define SCHED_FENCE() __builtin_amdgcn_sched_barrier(0)
#define ASM_TR(dst, addr, off) asm volatile("ds_read_b64_tr_b16 %0, %1 offset:%2" : "=v"(dst) : "v"(addr), "i"(off))
#define ASM_B128(dst, addr, off) asm volatile("ds_read_b128 %0, %1 offset:%2" : "=v"(dst) : "v"(addr), "i"(off))
#define ASM_B64(dst, addr, off) asm volatile("ds_read_b64 %0, %1 offset:%2" : "=v"(dst) : "v"(addr), "i"(off))

constexpr int G1_STAGE = 20480;
struct G1Frags { s16x8 x[4]; s16x4 wl[2][3], wh[2][3]; };

DEVI void g1_read(G1Frags& f, unsigned sb, unsigned xa, const unsigned (&wa)[3]) {
#pragma unroll
  for (int cc = 0; cc < 3; ++cc) {
    const unsigned a = sb + wa[cc];
    ASM_TR(f.wl[0][cc], a, 0); ASM_TR(f.wh[0][cc], a, 768);
    ASM_TR(f.wl[1][cc], a, 6144); ASM_TR(f.wh[1][cc], a, 6144 + 768);
  }
  const unsigned a = sb + xa;
  ASM_B128(f.x[0], a, 0); ASM_B128(f.x[1], a, 1024); ASM_B128(f.x[2], a, 2048); ASM_B128(f.x[3], a, 3072);
}
DEVI void g1_wait(G1Frags& f) {
  asm volatile("s_waitcnt lgkmcnt(0)"
               : "+v"(f.x[0]), "+v"(f.x[1]), "+v"(f.x[2]), "+v"(f.x[3]),
                 "+v"(f.wl[0][0]), "+v"(f.wl[0][1]), "+v"(f.wl[0][2]), "+v"(f.wl[1][0]), "+v"(f.wl[1][1]), "+v"(f.wl[1][2]),
                 "+v"(f.wh[0][0]), "+v"(f.wh[0][1]), "+v"(f.wh[0][2]), "+v"(f.wh[1][0]), "+v"(f.wh[1][1]), "+v"(f.wh[1][2])
               :: "memory");
}
template <bool DO_READ, bool DO_STAGE, class StageFn>
DEVI void g1_step(f32x4 (&acc)[2][3][4], const G1Frags& c, G1Frags& n, unsigned sb, unsigned xa,
                  const unsigned (&wa)[3], StageFn&& stage_piece) {
  const unsigned a0 = sb + wa[0], a1 = sb + wa[1], a2 = sb + wa[2], ax = sb + xa;
#pragma unroll
  for (int i = 0; i < 24; ++i) {
    const int cc = i >> 3, m = (i >> 2) & 1, tt = i & 3;
    acc[m][cc][tt] = mfma16(cat4(c.wl[m][cc], c.wh[m][cc]), c.x[tt], acc[m][cc][tt]);
    SCHED_FENCE();
    if (DO_READ) {
      if (i == 0) ASM_B128(n.x[0], ax, 0);
      if (i == 1) ASM_B128(n.x[1], ax, 1024);
      if (i == 2) ASM_B128(n.x[2], ax, 2048);
      if (i == 3) ASM_B128(n.x[3], ax, 3072);
      if (i == 4) ASM_TR(n.wl[0][0], a0, 0);
      if (i == 5) ASM_TR(n.wh[0][0], a0, 768);
      if (i == 6) ASM_TR(n.wl[1][0], a0, 6144);
      if (i == 7) ASM_TR(n.wh[1][0], a0, 6144 + 768);
      if (i == 8) ASM_TR(n.wl[0][1], a1, 0);
      if (i == 9) ASM_TR(n.wh[0][1], a1, 768);
      if (i == 10) ASM_TR(n.wl[1][1], a1, 6144);
      if (i == 11) ASM_TR(n.wh[1][1], a1, 6144 + 768);
      if (i == 12) ASM_TR(n.wl[0][2], a2, 0);
      if (i == 13) ASM_TR(n.wh[0][2], a2, 768);
      if (i == 14) ASM_TR(n.wl[1][2], a2, 6144);
      if (i == 15) ASM_TR(n.wh[1][2], a2, 6144 + 768);
    }
    if (DO_STAGE && i >= 2 && i < 22 && ((i - 2) & 3) == 0) stage_piece((i - 2) >> 2);
    SCHED_FENCE();
  }
}

DEVI void ffn_in(const u16* x, const u16* wg, const u16* wu, char* ws, LAS char* sm, int b, int w, int lane, u64 tag) {
  const int xcd = b & 7, li = b >> 3;
  const int panel = 8 * (xcd & 1) + (li & 7), col = 4 * (xcd >> 1) + (li >> 3);
  const int row0 = panel * 128, n0 = col * 96;
  const int wm = w >> 1, wn = w & 1, g = lane >> 4, l15 = lane & 15;
  const rsrc_t rx = mk_rsrc(x, SL * SD * 2);
  const rsrc_t rw = mk_rsrc(w < 2 ? wg : wu, SD * SF * 2);

  unsigned xo[2], wo[3];
#pragma unroll
  for (int i = 0; i < 2; ++i) {
    const int row = 16 * (2 * w + i) + (lane >> 2), ch = (lane & 3) ^ ((4 - g) & 3);
    xo[i] = (row0 + row) * (SD * 2) + ch * 16;
  }
#pragma unroll
  for (int i = 0; i < 3; ++i) {
    const int pp = (3 * w + i) % 6, o = pp * 1024 + lane * 16, k = o / 192, chp = (o - 192 * k) >> 4;
    const int n = (((chp >> 1) ^ ((k >> 3) & 1)) << 4) + (chp & 1) * 8;
    wo[i] = (k * SF + n0 + n) * 2;
  }
  auto piece = [&](int s, int i) {
    LAS char* sb = sm + (s & 3) * G1_STAGE;
    if (i < 2) dma16(rx, sb + (2 * w + i) * 1024, xo[i], s * 64);
    else dma16(rw, sb + 8192 + (3 * w + i - 2) * 1024, wo[i - 2], s * (32 * SF * 2));
  };
  auto stage = [&](int s) {
#pragma unroll
    for (int i = 0; i < 5; ++i) piece(s, i);
  };
  stage(0); stage(1); stage(2); stage(3);

  const unsigned xa = (64 * wm + l15) * 64 + ((g ^ ((4 - (l15 >> 2)) & 3)) << 4);
  unsigned wa[3];
#pragma unroll
  for (int cc = 0; cc < 3; ++cc)
    wa[cc] = 8192 + (8 * g + (l15 >> 2)) * 192 + (((3 * wn + cc) ^ (g & 1)) << 5) + (lane & 3) * 8;
  const unsigned sbase = (unsigned)(uintptr_t)sm;
  f32x4 acc[2][3][4];
#pragma unroll
  for (int m = 0; m < 2; ++m)
#pragma unroll
    for (int cc = 0; cc < 3; ++cc)
#pragma unroll
      for (int tt = 0; tt < 4; ++tt) acc[m][cc][tt] = f32x4{0.f, 0.f, 0.f, 0.f};

  G1Frags fa, fb;
  WAIT_VM(15); BARRIER();
  STAMP(0, 1);
  g1_read(fa, sbase, xa, wa);

#define G1_STEP(S, CUR, NXT, VM, RD, ST)                              \
  do {                                                                \
    g1_wait(CUR);                                                     \
    WAIT_VM(VM);                                                      \
    BARRIER();                                                        \
    SCHED_FENCE();                                                    \
    g1_step<RD, ST>(acc, CUR, NXT, sbase + (((S) + 1) & 3) * G1_STAGE, xa, wa, [&](int i_) { piece((S) + 4, i_); }); \
  } while (0)

  for (int s = 0; s < 16; s += 2) {
    G1_STEP(s, fa, fb, 10, true, true);
    G1_STEP(s + 1, fb, fa, 10, true, true);
  }
  G1_STEP(16, fa, fb, 10, true, false);
  G1_STEP(17, fb, fa, 5, true, false);
  G1_STEP(18, fa, fb, 0, true, false);
  G1_STEP(19, fb, fa, 0, false, false);
#undef G1_STEP
  STAMP(0, 2);

  const rsrc_t rh = mk_rsrc(ws + OFF_HF, SZ_HF);
  const int T0 = (row0 + 64 * wm) >> 4, F0 = 3 * col;
#pragma unroll
  for (int tt = 0; tt < 4; ++tt) {
    u32x2 hv[3];
#pragma unroll
    for (int cc = 0; cc < 3; ++cc) {
      float r[4];
#pragma unroll
      for (int i = 0; i < 4; ++i) {
        const float gv = acc[0][cc][tt][i], uv = acc[1][cc][tt][i];
        const float e = __builtin_amdgcn_exp2f(gv * -1.4426950408889634f);
        r[i] = gv * uv * __builtin_amdgcn_rcpf(1.0f + e);
      }
      hv[cc] = u32x2{pk_bf16(r[0], r[1]), pk_bf16(r[2], r[3])};
    }
    const unsigned base = ((T0 + tt) * 48 + F0) * 1024 + lane * 16;
    if (wn == 0) {
      bstore16_sc1(u32x4{hv[0].x, hv[0].y, hv[1].x, hv[1].y}, rh, base);
      __builtin_amdgcn_raw_buffer_store_b64(hv[2], rh, base + 1024, 0, 16);
    } else {
      __builtin_amdgcn_raw_buffer_store_b64(hv[0], rh, base + 1024 + 8, 0, 16);
      bstore16_sc1(u32x4{hv[1].x, hv[1].y, hv[2].x, hv[2].y}, rh, base + 2048);
    }
  }
  STAMP(0, 3);
  WAIT_VM(0);
  BARRIER();
  if (w == 0 && lane == 0) flag_store((gu64*)(ws + OFF_FLAG) + FL_H + panel * 16 + col, tag);
  STAMP(0, 4);
}

constexpr int F_RED = 0;
constexpr int F_AT = 61440;
constexpr int F_OUT = 71680;

DEVI void ffn_out(u16* out, char* ws, LAS char* sm, int b, int w, int lane, u64 tag) {
  const int xcd = b & 7, li = b >> 3;
  const int rt = 8 * (xcd & 3) + (li & 7), cg = 4 * (xcd >> 2) + (li >> 3);
  const int g = lane >> 4, l15 = lane & 15;
  gu64* fl = (gu64*)(ws + OFF_FLAG);
  const rsrc_t rh = mk_rsrc(ws + OFF_HF, SZ_HF);
  const rsrc_t rf = mk_rsrc(ws + OFF_WDF, SZ_WDF);
  const unsigned ho = (4 * rt * 48 + 12 * w) * 1024 + lane * 16;
  const unsigned wo = (5 * cg * 48 + 12 * w) * 1024 + lane * 16;
  f32x4 acc[4][5];
#pragma unroll
  for (int tt = 0; tt < 4; ++tt)
#pragma unroll
    for (int dd = 0; dd < 5; ++dd) acc[tt][dd] = f32x4{0.f, 0.f, 0.f, 0.f};
  u32x4 hb[3][4], wb[3][5];
  flags_wait(fl + FL_M + cg * 32 + 6 * w + (lane < 6 ? lane : 0), lane < 6, tag);
#pragma unroll
  for (int s = 0; s < 3; ++s)
#pragma unroll
    for (int dd = 0; dd < 5; ++dd) wb[s][dd] = bload16_sc1(rf, wo, dd * 48 * 1024 + s * 1024);
  flags_wait(fl + FL_H + (rt >> 1) * 16 + 4 * w + (lane & 3), true, tag);
  STAMP(0, 5);
#pragma unroll
  for (int s = 0; s < 3; ++s)
#pragma unroll
    for (int tt = 0; tt < 4; ++tt) hb[s][tt] = bload16_sc1(rh, ho, tt * 48 * 1024 + s * 1024);
  SCHED_FENCE();
#pragma unroll
  for (int s = 0; s < 12; ++s) {
    const int slot = s % 3;
#pragma unroll
    for (int dd = 0; dd < 5; ++dd) {
#pragma unroll
      for (int tt = 0; tt < 4; ++tt) {
        acc[tt][dd] = mfma16u(wb[slot][dd], hb[slot][tt], acc[tt][dd]);
        SCHED_FENCE();
        if (s + 3 < 12) {
          if (tt == 3) wb[slot][dd] = bload16_sc1(rf, wo, dd * 48 * 1024 + (s + 3) * 1024);
          if (dd == 4) hb[slot][tt] = bload16_sc1(rh, ho, tt * 48 * 1024 + (s + 3) * 1024);
        }
        SCHED_FENCE();
      }
    }
  }
  STAMP(0, 6);
  LAS char* red = sm + F_RED;
#pragma unroll
  for (int tt = 0; tt < 4; ++tt) {
    if (tt != w) {
      const int slot = (tt * 3 + (w > tt ? w - 1 : w)) * 5;
#pragma unroll
      for (int dd = 0; dd < 5; ++dd) *(LAS f32x4*)(red + (slot + dd) * 1024 + lane * 16) = acc[tt][dd];
    }
  }
  {
    gu64* p = lane < 32 ? fl + FL_A + cg * 32 + lane : fl + FL_M + (lane & 7) * 32 + rt;
    flags_wait(p, lane < 40, tag);
    const rsrc_t ra = mk_rsrc(ws + OFF_AT, SZ_AT);
    u32x4 av[3];
#pragma unroll
    for (int k = 0; k < 3; ++k) {
      const int c = 256 * k + 64 * w + lane;
      av[k] = bload16_sc1(ra, c < 640 ? (rt * SD + 80 * cg) * 128 + c * 16 : 0xfffffff0u, 0);
    }
#pragma unroll
    for (int k = 0; k < 3; ++k) {
      const int c = 256 * k + 64 * w + lane;
      if (c < 640) *(LAS u32x4*)(sm + F_AT + c * 16) = av[k];
    }
  }
  const float gt = __builtin_bit_cast(float, __hip_atomic_load((GAS unsigned*)(ws + OFF_GATE) + 64 * rt + 16 * w + l15,
                                                               __ATOMIC_RELAXED, __HIP_MEMORY_SCOPE_AGENT));
  WAIT_LGKM0();
  BARRIER();
  STAMP(0, 7);
  f32x4 bs[5];
#pragma unroll
  for (int dd = 0; dd < 5; ++dd) {
    f32x4 v = w == 0 ? acc[0][dd] : w == 1 ? acc[1][dd] : w == 2 ? acc[2][dd] : acc[3][dd];
#pragma unroll
    for (int k = 0; k < 3; ++k) v += *(LAS f32x4*)(red + ((w * 3 + k) * 5 + dd) * 1024 + lane * 16);
    bs[dd] = v;
  }
  const float og = rnd_bf(1.0f - gt);
  LAS char* os = sm + F_OUT + w * 2560;
#pragma unroll
  for (int dd = 0; dd < 5; ++dd) {
    const s16x4 av = lds_tr(sm + F_AT + (16 * dd + 4 * g + (l15 >> 2)) * 128 + (16 * w + 4 * (lane & 3)) * 2);
    float r[4];
#pragma unroll
    for (int e = 0; e < 4; ++e) {
      const float a = bf2f((u16)av[e]);
      r[e] = rnd_bf(gt * a) + rnd_bf(og * rnd_bf(bs[dd][e]));
    }
    *(LAS u32x2*)(os + l15 * 160 + dd * 32 + g * 8) = u32x2{pk_bf16(r[0], r[1]), pk_bf16(r[2], r[3])};
  }
  WAIT_LGKM0();
  const rsrc_t ro = mk_rsrc(out, SL * SD * 2);
#pragma unroll
  for (int k = 0; k < 3; ++k) {
    const int c = 64 * k + lane;
    if (c < 160) {
      const int row = c / 10, ch = c - 10 * row;
      const u32x4 v = *(LAS u32x4*)(os + c * 16);
      __builtin_amdgcn_raw_buffer_store_b128(v, ro, ((64 * rt + 16 * w + row) * SD + 80 * cg) * 2 + ch * 16, 0, 0);
    }
  }
  STAMP(0, 8);
}

constexpr int CP_STRIDE = 4416;
constexpr int CP_CH = 4 * CP_STRIDE;
constexpr int U_BLK = 160, U_CH = 33 * U_BLK;
constexpr int C_CP = 0;
constexpr int C_U = C_CP + 3 * CP_CH;
constexpr int C_ST = C_U + 3 * U_CH;
constexpr int C_SYNC = C_ST + 4 * 2048;
constexpr int C_MISC = C_U;

DEVI f32x2 cmul(f32x2 a, f32x2 b) {
  const f32x2 t = f32x2{a.x, a.x} * b;
  return __builtin_elementwise_fma(f32x2{a.y, a.y}, f32x2{-b.y, b.x}, t);
}
DEVI f32x2 cexp_pos(float zr2, float zi2, float p) {
  const float e = __builtin_amdgcn_exp2f(zr2 * p);
  const float th = __builtin_amdgcn_fractf(zi2 * p);
  return f32x2{e * __builtin_amdgcn_cosf(th), e * __builtin_amdgcn_sinf(th)};
}
constexpr int KG_ROW = 288;

DEVI void kgen_wave(const u16* logA, const u16* Aim, const u16* Cre, const u16* Cim, const u16* logdt,
                    LAS char* sm, LAS char* kqp, LAS unsigned* misc_free, int d, int lane) {
  const int g = lane >> 4, l15 = lane & 15;
  u16 pla[3], pai[3], pcr[3], pci[3];
#pragma unroll
  for (int c = 0; c < 3; ++c) {
    const int idx = d * SN + 64 * c + lane;
    pla[c] = logA[idx]; pai[c] = Aim[idx]; pcr[c] = Cre[idx]; pci[c] = Cim[idx];
  }
  const float dt = rnd_bf(expf(bf2f(logdt[d])));
  f32x4 acc[3] = {f32x4{0.f, 0.f, 0.f, 0.f}, f32x4{0.f, 0.f, 0.f, 0.f}, f32x4{0.f, 0.f, 0.f, 0.f}};
  LAS char* tw = sm + lane * 4;
  auto putx = [&](int row, f32x2 v) { *(LAS unsigned*)(tw + row * KG_ROW) = pk_bf16(v.x, -v.y); };
  auto puty = [&](int row, f32x2 v) { *(LAS unsigned*)(tw + (41 + row) * KG_ROW) = pk_bf16(v.x, v.y); };
#pragma unroll 1
  for (int c = 0; c < 3; ++c) {
    const u16 la = c == 0 ? pla[0] : c == 1 ? pla[1] : pla[2];
    const u16 ai = c == 0 ? pai[0] : c == 1 ? pai[1] : pai[2];
    const u16 cr = c == 0 ? pcr[0] : c == 1 ? pcr[1] : pcr[2];
    const u16 ci = c == 0 ? pci[0] : c == 1 ? pci[1] : pci[2];
    const float zr = -rnd_bf(expf(bf2f(la))) * dt, zi = bf2f(ai) * dt;
    const float zr2 = zr * 1.4426950408889634f, zi2 = zi * 0.15915494309189535f;
    const f32x2 C = {bf2f(cr) * dt, bf2f(ci) * dt};
    const f32x2 w1 = cexp_pos(zr2, zi2, 1.f), w128 = cexp_pos(zr2, zi2, 128.f);
    const f32x2 w2 = cmul(w1, w1), w4 = cmul(w2, w2), w8 = cmul(w4, w4);
    const f32x2 w256 = cmul(w128, w128), w512 = cmul(w256, w256), w1024 = cmul(w512, w512);
    {
      f32x2 y = {1.f, 0.f};
      puty(0, y);
      y = w8; puty(1, y);
#pragma unroll
      for (int bb = 2; bb < 16; ++bb) { y = cmul(y, w8); puty(bb, y); }
    }
    {
      f32x2 v = C;
#pragma unroll
      for (int k = 0; k < 8; ++k) {
        if (k) v = cmul(v, w1);
        putx(k, v); putx(8 + k, cmul(v, w128));
      }
    }
    {
      f32x2 v = cmul(C, w256);
#pragma unroll
      for (int k = 0; k < 4; ++k) {
        if (k) v = cmul(v, w2);
        putx(16 + k, v); putx(20 + k, cmul(v, w128));
      }
    }
    {
      f32x2 v0 = cmul(C, w512), v1 = cmul(v0, w4);
#pragma unroll
      for (int k = 0; k < 4; ++k) {
        if (k) { v0 = cmul(v0, w128); v1 = cmul(v1, w128); }
        putx(24 + 2 * k, v0); putx(25 + 2 * k, v1);
      }
    }
    {
      f32x2 v = cmul(C, w1024);
#pragma unroll
      for (int k = 0; k < 9; ++k) {
        if (k) v = cmul(v, w128);
        putx(32 + k, v);
      }
    }
    WAIT_LGKM0();
#pragma unroll
    for (int s = 0; s < 4; ++s) {
      const unsigned o = l15 * KG_ROW + 64 * s + 16 * g;
      const s16x8 yb = lds_b128(sm + 41 * KG_ROW + o);
#pragma unroll
      for (int i = 0; i < 3; ++i) acc[i] = mfma16(lds_b128(sm + i * 16 * KG_ROW + o), yb, acc[i]);
    }
    WAIT_LGKM0();
  }
  lds_wait(misc_free, 1u);
  LAS float* kq = (LAS float*)kqp;
#pragma unroll
  for (int i = 0; i < 4; ++i) {
    const int r = 4 * g + i;
    kq[(r & 7) + 128 * (r >> 3) + 8 * l15] = 2.f * acc[0][i];
    const int rr = r - 8;
    const int i1 = r < 8 ? 256 + (r & 3) + 64 * (r >> 2) + 4 * l15 : 384 + (rr & 1) + 32 * (rr >> 1) + 2 * l15;
    kq[i1] = 2.f * acc[1][i];
    if (r < 8) kq[512 + 16 * r + l15] = 2.f * acc[2][i];
    else if (r == 8 && l15 == 0) kq[640] = 2.f * acc[2][i];
  }
  WAIT_LGKM0();
  u32x4 kv[5];
#pragma unroll
  for (int k = 0; k < 4; ++k) {
    const int i0 = 8 * (64 * k + lane);
#pragma unroll
    for (int j = 0; j < 8; j += 2) {
      const int la = 2047 - i0 - j, lb = la - 1;
      const unsigned bits = pk_bf16((float)la, (float)lb);
      const int ia = la < 256 ? la : (int)(bits & 0xffffu) - 17024;
      const int ib = lb < 256 ? lb : (int)(bits >> 16) - 17024;
      kv[k][j >> 1] = pk_bf16(kq[ia], kq[ib]);
    }
    *(LAS u32x4*)(sm + (64 * k + lane) * 16) = kv[k];
  }
  kv[4] = u32x4{0, 0, 0, 0};
  if (lane < 9) *(LAS u32x4*)(sm + (256 + lane) * 16) = kv[4];
  WAIT_LGKM0();
#pragma unroll
  for (int k = 0; k < 5; ++k) {
    const int m = 64 * k + lane;
    if (m < 264) {
      const u32x4 a = kv[k];
      const u32x2 n = *(LAS u32x2*)(sm + (m + 1) * 16);
      *(LAS u32x4*)(sm + CP_STRIDE + m * 16) =
          u32x4{__builtin_amdgcn_alignbyte(a.y, a.x, 2), __builtin_amdgcn_alignbyte(a.z, a.y, 2),
                __builtin_amdgcn_alignbyte(a.w, a.z, 2), __builtin_amdgcn_alignbyte(n.x, a.w, 2)};
      *(LAS u32x4*)(sm + 2 * CP_STRIDE + m * 16) = u32x4{a.y, a.z, a.w, n.x};
      *(LAS u32x4*)(sm + 3 * CP_STRIDE + m * 16) =
          u32x4{__builtin_amdgcn_alignbyte(a.z, a.y, 2), __builtin_amdgcn_alignbyte(a.w, a.z, 2),
                __builtin_amdgcn_alignbyte(n.x, a.w, 2), __builtin_amdgcn_alignbyte(n.y, n.x, 2)};
    }
  }
  WAIT_LGKM0();
}

DEVI void u_fill(char* ws, LAS char* ub, int d, int cg, int lane, u64 tag) {
  flags_wait((gu64*)(ws + OFF_FLAG) + FL_M + cg * 32 + (lane & 31), true, tag);
  const rsrc_t rut = mk_rsrc(ws + OFF_UT, SZ_UT);
  u32x4 uv[4];
#pragma unroll
  for (int k = 0; k < 4; ++k) uv[k] = bload16_sc1(rut, d * (SL * 2) + (64 * k + lane) * 16, 0);
  if (lane < 10) *(LAS u32x4*)(ub + lane * 16) = u32x4{0, 0, 0, 0};
#pragma unroll
  for (int k = 0; k < 4; ++k) {
    const int m = 64 * k + lane;
    *(LAS u32x4*)(ub + (1 + (m >> 3)) * U_BLK + (m & 7) * 16) = uv[k];
  }
  WAIT_LGKM0();
}

DEVI void misc_wave(const u16* u, const u16* wd, const u16* gw, char* ws, LAS char* sm, LAS unsigned* misc_free,
                    int i, int lane, u64 tag) {
  const int g = lane >> 4, l15 = lane & 15, q = l15 >> 2, p = lane & 3;
  const rsrc_t ru = mk_rsrc(u, SL * SD * 2);
  const rsrc_t rwd = mk_rsrc(wd, SF * SD * 2);
  const int rt = i >> 3, cg = i & 7;
#pragma unroll
  for (int k = 0; k < 10; ++k) {
    const int c = 64 * k + lane, row = c / 10, ch = c - 10 * row;
    dma16(ru, sm + k * 1024, ((64 * rt + row) * SD + 80 * cg) * 2 + ch * 16, 0);
  }
  if (rt < 24) {
#pragma unroll
    for (int k = 0; k < 10; ++k) {
      const int c = 64 * k + lane, row = c / 10, ch = c - 10 * row;
      dma16(rwd, sm + 10240 + k * 1024, ((64 * rt + row) * SD + 80 * cg) * 2 + ch * 16, 0);
    }
  }
  {
    const int row = 8 * i + (lane >> 3), seg = lane & 7;
    const rsrc_t rg = mk_rsrc(gw, SD * 2);
    float s = 0.f;
#pragma unroll
    for (int k = 0; k < 10; ++k) {
      const u32x4 xv = bload16(ru, row * (SD * 2) + (seg + 8 * k) * 16, 0);
      const u32x4 gv = bload16(rg, (seg + 8 * k) * 16, 0);
#pragma unroll
      for (int j = 0; j < 4; ++j) {
        s = fmaf(bf_lo(xv[j]), bf_lo(gv[j]), s);
        s = fmaf(bf_hi(xv[j]), bf_hi(gv[j]), s);
      }
    }
    s += __shfl_xor(s, 1); s += __shfl_xor(s, 2); s += __shfl_xor(s, 4);
    s = rnd_bf(s);
    const float gt = rnd_bf(1.0f / (1.0f + expf(-s)));
    if (seg == 0)
      __hip_atomic_store((GAS unsigned*)(ws + OFF_GATE) + row, __builtin_bit_cast(unsigned, gt), __ATOMIC_RELAXED, __HIP_MEMORY_SCOPE_AGENT);
  }
  WAIT_VM(0);
  const rsrc_t rut = mk_rsrc(ws + OFF_UT, SZ_UT);
#pragma unroll
  for (int c = 0; c < 5; ++c)
#pragma unroll
    for (int h = 0; h < 2; ++h) {
      const int t0 = 32 * h + 8 * g;
      LAS char* a = sm + (t0 + q) * 160 + c * 32 + p * 8;
      const s16x8 v = cat4(lds_tr(a), lds_tr(a + 4 * 160));
      bstore16_sc1(__builtin_bit_cast(u32x4, v), rut, ((80 * cg + 16 * c + l15) * SL + 64 * rt + t0) * 2);
    }
  if (rt < 24) {
    const rsrc_t rf = mk_rsrc(ws + OFF_WDF, SZ_WDF);
#pragma unroll
    for (int fb = 0; fb < 2; ++fb)
#pragma unroll
      for (int c = 0; c < 5; ++c) {
        LAS char* a = sm + 10240 + (32 * fb + 4 * g + q) * 160 + c * 32 + p * 8;
        const s16x8 v = cat4(lds_tr(a), lds_tr(a + 16 * 160));
        bstore16_sc1(__builtin_bit_cast(u32x4, v), rf, ((5 * cg + c) * 48 + 2 * rt + fb) * 1024 + lane * 16);
      }
  }
  WAIT_LGKM0();
  if (lane == 0) __hip_atomic_store(misc_free, 1u, __ATOMIC_RELAXED, __HIP_MEMORY_SCOPE_WORKGROUP);
  WAIT_VM(0);
  if (lane == 0) flag_store((gu64*)(ws + OFF_FLAG) + FL_M + cg * 32 + rt, tag);
}

template <int NMT>
DEVI void conv_store(const f32x4 (&acc)[NMT], int mt0, int j, int d, float dsk, char* ws, LAS char* ub, LAS char* st, int lane) {
  const int g = lane >> 4, l15 = lane & 15;
#pragma unroll
  for (int m = 0; m < NMT; ++m)
    *(LAS u32x2*)(st + l15 * 128 + (mt0 + m) * 32 + g * 8) =
        u32x2{pk_bf16(acc[m][0], acc[m][1]), pk_bf16(acc[m][2], acc[m][3])};
  WAIT_LGKM0();
  const rsrc_t ra = mk_rsrc(ws + OFF_AT, SZ_AT);
  constexpr int PER = NMT * 2;
#pragma unroll
  for (int k = 0; k < 16 * PER / 64; ++k) {
    const int c = 64 * k + lane, blk = c / PER, pc = 2 * mt0 + c % PER;
    const u32x4 yv = *(LAS u32x4*)(st + blk * 128 + pc * 16);
    const u32x4 uv = *(LAS u32x4*)(ub + (1 + 16 * j + blk) * U_BLK + pc * 16);
    u32x4 o;
#pragma unroll
    for (int e = 0; e < 4; ++e) {
      const unsigned sk = pk_bf16(bf_lo(uv[e]) * dsk, bf_hi(uv[e]) * dsk);
      o[e] = pk_bf16(bf_lo(yv[e]) + bf_lo(sk), bf_hi(yv[e]) + bf_hi(sk));
    }
    bstore16_sc1(o, ra, ((16 * j + blk) * SD + d) * 128 + pc * 16);
  }
  WAIT_LGKM0();
}

struct TFrag { s16x4 lo, hi; };
DEVI s16x8 tf(const TFrag& f) { return cat4(f.lo, f.hi); }
DEVI unsigned ublk(unsigned ub, int ai) { return ub + (unsigned)(ai > 0 ? ai : 0) * U_BLK; }

struct C4Set { TFrag a[4]; s16x8 b[2]; };
DEVI void c4_wait(C4Set& f) {
  asm volatile("s_waitcnt lgkmcnt(0)"
               : "+v"(f.a[0].lo), "+v"(f.a[0].hi), "+v"(f.a[1].lo), "+v"(f.a[1].hi), "+v"(f.a[2].lo), "+v"(f.a[2].hi),
                 "+v"(f.a[3].lo), "+v"(f.a[3].hi), "+v"(f.b[0]), "+v"(f.b[1]) :: "memory");
}
DEVI void c4_step(f32x4 (&acc)[4], const C4Set& c, const TFrag& m2, const TFrag& m1, C4Set& n, unsigned aa, unsigned bb) {
#define C4(i, A, B, RDSTMT) acc[i] = mfma16(tf(A), B, acc[i]); SCHED_FENCE(); { RDSTMT; } SCHED_FENCE();
  C4(0, m2, c.b[1], ASM_B128(n.b[0], bb, 0); ASM_B128(n.b[1], bb, 64))
  C4(1, m1, c.b[1], ASM_B64(n.a[0].lo, aa, 96); ASM_B64(n.a[0].hi, aa, 104))
  C4(2, c.a[0], c.b[1], ASM_B64(n.a[1].lo, aa, 64); ASM_B64(n.a[1].hi, aa, 72))
  C4(3, c.a[1], c.b[1], ASM_B64(n.a[2].lo, aa, 32))
  C4(0, c.a[0], c.b[0], ASM_B64(n.a[2].hi, aa, 40))
  C4(1, c.a[1], c.b[0], ASM_B64(n.a[3].lo, aa, 0))
  C4(2, c.a[2], c.b[0], ASM_B64(n.a[3].hi, aa, 8))
  C4(3, c.a[3], c.b[0], )
#undef C4
}
DEVI void conv_unit4(int j, int d, float dsk, char* ws, LAS char* cp, LAS char* ub, LAS char* st, int lane) {
  const int g = lane >> 4, r = lane & 15;
  const int i0 = 2047 - r + 8 * g, c = i0 & 3;
  unsigned aa = (unsigned)(uintptr_t)cp + c * CP_STRIDE + (i0 - c) * 2 - 96;
  const unsigned ubg = (unsigned)(uintptr_t)ub + 16 * g;
  int ai = 1 + 16 * j + r;
  f32x4 acc[4] = {f32x4{0, 0, 0, 0}, f32x4{0, 0, 0, 0}, f32x4{0, 0, 0, 0}, f32x4{0, 0, 0, 0}};
  C4Set fa, fb;
  TFrag m2, m1;
  {
    const unsigned bb = ublk(ubg, ai);
    ASM_B64(m2.lo, aa, 160); ASM_B64(m2.hi, aa, 168); ASM_B64(m1.lo, aa, 128); ASM_B64(m1.hi, aa, 136);
    ASM_B128(fa.b[0], bb, 0); ASM_B128(fa.b[1], bb, 64);
    ASM_B64(fa.a[0].lo, aa, 96); ASM_B64(fa.a[0].hi, aa, 104); ASM_B64(fa.a[1].lo, aa, 64); ASM_B64(fa.a[1].hi, aa, 72);
    ASM_B64(fa.a[2].lo, aa, 32); ASM_B64(fa.a[2].hi, aa, 40); ASM_B64(fa.a[3].lo, aa, 0); ASM_B64(fa.a[3].hi, aa, 8);
    asm volatile("s_waitcnt lgkmcnt(0)" : "+v"(m2.lo), "+v"(m2.hi), "+v"(m1.lo), "+v"(m1.hi) :: "memory");
  }
  const int nd = 16 * (j + 1);
  for (int dl = 0; dl < nd; dl += 2) {
    c4_wait(fa);
    aa -= 128; ai -= 1;
    c4_step(acc, fa, m2, m1, fb, aa, ublk(ubg, ai));
    c4_wait(fb);
    aa -= 128; ai -= 1;
    c4_step(acc, fb, fa.a[2], fa.a[3], fa, aa, ublk(ubg, ai));
    m2 = fb.a[2]; m1 = fb.a[3];
  }
  c4_wait(fa);
  conv_store<4>(acc, 0, j, d, dsk, ws, ub, st, lane);
}

struct C2Set { TFrag a[4]; s16x8 p[2], q[2]; };
DEVI void c2_wait(C2Set& f) {
  asm volatile("s_waitcnt lgkmcnt(0)"
               : "+v"(f.a[0].lo), "+v"(f.a[0].hi), "+v"(f.a[1].lo), "+v"(f.a[1].hi), "+v"(f.a[2].lo), "+v"(f.a[2].hi),
                 "+v"(f.a[3].lo), "+v"(f.a[3].hi), "+v"(f.p[0]), "+v"(f.p[1]), "+v"(f.q[0]), "+v"(f.q[1]) :: "memory");
}
template <bool BOTH>
DEVI void c2_step(f32x4 (&c0)[2], f32x4 (&c1)[2], const C2Set& c, C2Set& n, unsigned aa, unsigned bp, unsigned bq) {
#define C2(ACC, A, B, RDSTMT) ACC = mfma16(tf(A), B, ACC); SCHED_FENCE(); { RDSTMT; } SCHED_FENCE();
  C2(c1[0], c.a[0], c.p[0], ASM_B128(n.p[0], bp, 0); ASM_B128(n.p[1], bp, 64); ASM_B64(n.a[0].lo, aa, 32))
  C2(c1[1], c.a[1], c.p[0], ASM_B64(n.a[0].hi, aa, 40); ASM_B64(n.a[1].lo, aa, 0); ASM_B64(n.a[1].hi, aa, 8))
  C2(c1[0], c.a[2], c.p[1], ASM_B64(n.a[2].lo, aa, 96); ASM_B64(n.a[2].hi, aa, 104))
  C2(c1[1], c.a[3], c.p[1], ASM_B64(n.a[3].lo, aa, 64); ASM_B64(n.a[3].hi, aa, 72))
  if (BOTH) {
    C2(c0[0], c.a[0], c.q[0], ASM_B128(n.q[0], bq, 0))
    C2(c0[1], c.a[1], c.q[0], ASM_B128(n.q[1], bq, 64))
    C2(c0[0], c.a[2], c.q[1], )
    C2(c0[1], c.a[3], c.q[1], )
  }
#undef C2
}
DEVI void conv_unit2(int mt0, int d, float dsk, char* ws, LAS char* cp, LAS char* ub, LAS char* st, int lane) {
  const int g = lane >> 4, r = lane & 15;
  const int i0 = 2047 - r + 8 * g, c = i0 & 3;
  unsigned aa = (unsigned)(uintptr_t)cp + c * CP_STRIDE + (i0 - c) * 2 - 32 * mt0 - 32;
  const unsigned ubg = (unsigned)(uintptr_t)ub + 16 * g;
  int ai = 1 + r;
  f32x4 c0[2] = {f32x4{0, 0, 0, 0}, f32x4{0, 0, 0, 0}}, c1[2] = {f32x4{0, 0, 0, 0}, f32x4{0, 0, 0, 0}};
  C2Set fa, fb;
  {
    const unsigned bp = ublk(ubg, ai + 16), bq = ublk(ubg, ai);
    ASM_B128(fa.p[0], bp, 0); ASM_B128(fa.p[1], bp, 64); ASM_B128(fa.q[0], bq, 0); ASM_B128(fa.q[1], bq, 64);
    ASM_B64(fa.a[0].lo, aa, 32); ASM_B64(fa.a[0].hi, aa, 40); ASM_B64(fa.a[1].lo, aa, 0); ASM_B64(fa.a[1].hi, aa, 8);
    ASM_B64(fa.a[2].lo, aa, 96); ASM_B64(fa.a[2].hi, aa, 104); ASM_B64(fa.a[3].lo, aa, 64); ASM_B64(fa.a[3].hi, aa, 72);
  }
  fb.q[0] = s16x8{0, 0, 0, 0, 0, 0, 0, 0}; fb.q[1] = fb.q[0];
  for (int dl = 0; dl < 16; dl += 2) {
    c2_wait(fa);
    aa -= 128; ai -= 1;
    c2_step<true>(c0, c1, fa, fb, aa, ublk(ubg, ai + 16), ublk(ubg, ai));
    c2_wait(fb);
    aa -= 128; ai -= 1;
    c2_step<true>(c0, c1, fb, fa, aa, ublk(ubg, ai + 16), ublk(ubg, ai));
  }
  for (int dl = 16; dl < 32; dl += 2) {
    c2_wait(fa);
    aa -= 128; ai -= 1;
    c2_step<false>(c0, c1, fa, fb, aa, ublk(ubg, ai + 16), 0);
    c2_wait(fb);
    aa -= 128; ai -= 1;
    c2_step<false>(c0, c1, fb, fa, aa, ublk(ubg, ai + 16), 0);
  }
  c2_wait(fa);
  conv_store<2>(c0, mt0, 0, d, dsk, ws, ub, st, lane);
  conv_store<2>(c1, mt0, 1, d, dsk, ws, ub, st, lane);
}

DEVI void c_role(const u16* u, const u16* logA, const u16* Aim, const u16* Cre, const u16* Cim, const u16* logdt,
                 const u16* dskip, const u16* wd, const u16* gw, char* ws, LAS char* sm, int i, int w, int lane, u64 tag) {
  const int xcd = i & 7, li = i >> 3;
  const int rt = 8 * (xcd & 3) + (li & 7), cg = 4 * (xcd >> 2) + (li >> 3);
  const int pi = rt >> 1, odd = rt & 1;
  const int dX = 80 * cg + 5 * pi + (odd ? 3 : 0), dY = dX + 1, dZ = 80 * cg + 5 * pi + 2;
  LAS unsigned* sync = (LAS unsigned*)(sm + C_SYNC);
  if (threadIdx.x < 4) sync[threadIdx.x] = 0u;
  WAIT_LGKM0();
  BARRIER();
  LAS char* st = sm + C_ST + w * 2048;
  __builtin_amdgcn_s_setprio(PRIO_KGEN);
  if (w < 3) {
    const int d = w == 0 ? dX : w == 1 ? dY : dZ;
    LAS char* cp = sm + C_CP + w * CP_CH;
    LAS char* ub = sm + C_U + w * U_CH;
    const float dsk = bf2f(dskip[d]);
    kgen_wave(logA, Aim, Cre, Cim, logdt, cp, ub, sync + 3, d, lane);
    STAMP(1, 1);
    u_fill(ws, ub, d, cg, lane, tag);
    if (w < 2 && lane == 0) __hip_atomic_store(sync + w, 1u, __ATOMIC_RELAXED, __HIP_MEMORY_SCOPE_WORKGROUP);
    STAMP(1, 2);
    __builtin_amdgcn_s_setprio(PRIO_CONV);
    if (w < 2) conv_unit4(1, d, dsk, ws, cp, ub, st, lane);
    else conv_unit2(odd ? 2 : 0, d, dsk, ws, cp, ub, st, lane);
  } else {
    const float dx = bf2f(dskip[dX]), dy = bf2f(dskip[dY]);
    misc_wave(u, wd, gw, ws, sm + C_MISC, sync + 3, i, lane, tag);
    STAMP(1, 1);
    lds_wait(sync + 0, 1u);
    STAMP(1, 2);
    __builtin_amdgcn_s_setprio(PRIO_CONV);
    conv_unit4(0, dX, dx, ws, sm + C_CP, sm + C_U, st, lane);
    lds_wait(sync + 1, 1u);
    conv_unit4(0, dY, dy, ws, sm + C_CP + CP_CH, sm + C_U + U_CH, st, lane);
  }
  STAMP(1, 3);
  WAIT_VM(0);
  unsigned arrived = 0;
  if (lane == 0) arrived = __hip_atomic_fetch_add(sync + 2, 1u, __ATOMIC_RELAXED, __HIP_MEMORY_SCOPE_WORKGROUP);
  arrived = __builtin_amdgcn_readfirstlane(arrived);
  gu64* fl = (gu64*)(ws + OFF_FLAG);
  if (arrived == 3u) {
    if (lane == 0) flag_store(fl + FL_A + cg * 32 + rt, tag);
    STAMP(1, 4);
    if (i == 0) {
#pragma unroll 1
      for (int k = 0; k < 4; ++k) {
        flags_wait(fl + FL_H + 64 * k + lane, true, tag);
        flags_wait(fl + FL_A + 64 * k + lane, true, tag);
      }
      if (lane == 0) flag_store(fl + FL_E, tag);
    }
  }
}

__global__ __launch_bounds__(256, 2) void gated_mix_fused(
    const u16* __restrict__ u, const u16* __restrict__ logA, const u16* __restrict__ Aim,
    const u16* __restrict__ Cre, const u16* __restrict__ Cim, const u16* __restrict__ logdt,
    const u16* __restrict__ dskip, const u16* __restrict__ wg, const u16* __restrict__ wu,
    const u16* __restrict__ wd, const u16* __restrict__ gw, char* __restrict__ ws, u16* __restrict__ out) {
  extern __shared__ __attribute__((aligned(16))) char smem[];
  LAS char* sm = (LAS char*)smem;
  const int b = blockIdx.x, lane = threadIdx.x & 63;
  const int w = __builtin_amdgcn_readfirstlane(threadIdx.x >> 6);
  const u64 tag = flag_load((gu64*)(ws + OFF_FLAG) + FL_E) + 1;
  if (b < 256) {
    STAMP(0, 0);
    __builtin_amdgcn_s_setprio(PRIO_G1);
    ffn_in(u, wg, wu, ws, sm, b, w, lane, tag);
    __builtin_amdgcn_s_setprio(PRIO_G2);
    ffn_out(out, ws, sm, b, w, lane, tag);
  } else {
    STAMP(1, 0);
    c_role(u, logA, Aim, Cre, Cim, logdt, dskip, wd, gw, ws, sm, b - 256, w, lane, tag);
  }
}

extern "C" void kernel_launch(void* const* d_in, const int* in_sizes, int n_in, void* d_out, int out_size,
                              void* d_ws, size_t ws_size, hipStream_t stream) {
  const u16* u = (const u16*)d_in[0];
  const u16* logA = (const u16*)d_in[1];
  const u16* Aim = (const u16*)d_in[2];
  const u16* Cre = (const u16*)d_in[3];
  const u16* Cim = (const u16*)d_in[4];
  const u16* logdt = (const u16*)d_in[5];
  const u16* dskip = (const u16*)d_in[6];
  const u16* wg = (const u16*)d_in[8];
  const u16* wu = (const u16*)d_in[9];
  const u16* wd = (const u16*)d_in[10];
  const u16* gw = (const u16*)d_in[11];
  (void)hipFuncSetAttribute((const void*)gated_mix_fused, hipFuncAttributeMaxDynamicSharedMemorySize, LDS_BYTES);
  gated_mix_fused<<<512, 256, LDS_BYTES, stream>>>(u, logA, Aim, Cre, Cim, logdt, dskip, wg, wu, wd, gw, (char*)d_ws, (u16*)d_out);
}
```

```cpp
#include <hip/hip_runtime.h>
#include <hip/hip_bf16.h>
#include <stdint.h>

#define LAS __attribute__((address_space(3)))
#define GAS __attribute__((address_space(1)))
#define DEVI __device__ __forceinline__

typedef unsigned short u16;
typedef unsigned long long u64;
typedef short s16x8 __attribute__((ext_vector_type(8)));
typedef short s16x4 __attribute__((ext_vector_type(4)));
typedef __bf16 b16x8 __attribute__((ext_vector_type(8)));
typedef __bf16 b16x2 __attribute__((ext_vector_type(2)));
typedef _Float16 f16x2 __attribute__((ext_vector_type(2)));
typedef _Float16 f16x8 __attribute__((ext_vector_type(8)));
typedef float f32x4 __attribute__((ext_vector_type(4)));
typedef float f32x2 __attribute__((ext_vector_type(2)));
typedef float f32x16 __attribute__((ext_vector_type(16)));
typedef unsigned u32x4 __attribute__((ext_vector_type(4)));
typedef unsigned u32x2 __attribute__((ext_vector_type(2)));
typedef __amdgpu_buffer_rsrc_t rsrc_t;
typedef GAS u64 gu64;

constexpr int SL = 2048, SD = 640, SN = 192, SF = 1536;
constexpr unsigned OFF_HF = 0, SZ_HF = SL * SF * 2;
constexpr unsigned OFF_WDF = OFF_HF + SZ_HF, SZ_WDF = SF * SD * 2;
constexpr unsigned OFF_UT = OFF_WDF + SZ_WDF, SZ_UT = SL * SD * 2;
constexpr unsigned OFF_AT = OFF_UT + SZ_UT, SZ_AT = SL * SD * 2;
constexpr unsigned OFF_GATE = OFF_AT + SZ_AT, SZ_GATE = SL * 4;
constexpr unsigned OFF_FLAG = OFF_GATE + SZ_GATE;
constexpr int FL_E = 0, FL_H = 32, FL_M = FL_H + 256, FL_A = FL_M + 256, FL_P = FL_A + 256;
constexpr unsigned OFF_STAMP = 32u << 20;
constexpr int LDS_BYTES = 81920;
#define PRIO_KGEN 1
#define PRIO_CONV 0
#define PRIO_EPI 0
#define PRIO_G1 0
#define PRIO_G2 0

#define STAMP_CLOCK() __builtin_amdgcn_s_memrealtime()
#define STAMP(k, j) do {} while (0)

DEVI rsrc_t mk_rsrc(const void* p, unsigned bytes) {
  return __builtin_amdgcn_make_buffer_rsrc((void*)p, 0, bytes, 0x00020000);
}
DEVI f32x4 mfma16(s16x8 a, s16x8 b, f32x4 c) {
  return __builtin_amdgcn_mfma_f32_16x16x32_bf16(__builtin_bit_cast(b16x8, a), __builtin_bit_cast(b16x8, b), c, 0, 0, 0);
}
DEVI f32x4 mfma16u(u32x4 a, u32x4 b, f32x4 c) {
  return __builtin_amdgcn_mfma_f32_16x16x32_bf16(__builtin_bit_cast(b16x8, a), __builtin_bit_cast(b16x8, b), c, 0, 0, 0);
}
DEVI f32x16 mfma32(s16x8 a, s16x8 b, f32x16 c) {
  return __builtin_amdgcn_mfma_f32_32x32x16_bf16(__builtin_bit_cast(b16x8, a), __builtin_bit_cast(b16x8, b), c, 0, 0, 0);
}
DEVI void dma16(rsrc_t r, LAS char* dst, unsigned voff, unsigned soff) {
  __builtin_amdgcn_raw_ptr_buffer_load_lds(r, (LAS void*)dst, 16, voff, soff, 0, 0);
}
DEVI u32x4 bload16(rsrc_t r, unsigned voff, unsigned soff) {
  return __builtin_bit_cast(u32x4, __builtin_amdgcn_raw_buffer_load_b128(r, voff, soff, 0));
}
DEVI u32x4 bload16_sc1(rsrc_t r, unsigned voff, unsigned soff) {
  return __builtin_bit_cast(u32x4, __builtin_amdgcn_raw_buffer_load_b128(r, voff, soff, 16));
}
DEVI void bstore16_sc1(u32x4 v, rsrc_t r, unsigned voff) { __builtin_amdgcn_raw_buffer_store_b128(v, r, voff, 0, 16); }
DEVI u64 flag_load(gu64* p) { return __hip_atomic_load(p, __ATOMIC_RELAXED, __HIP_MEMORY_SCOPE_AGENT); }
DEVI void flag_store(gu64* p, u64 v) { __hip_atomic_store(p, v, __ATOMIC_RELAXED, __HIP_MEMORY_SCOPE_AGENT); }
DEVI void flags_wait(gu64* p, bool vote, u64 tag) {
#pragma unroll 1
  for (unsigned spins = 0; spins < (1u << 18); ++spins) {
    const u64 v = flag_load(p);
    if (__all(!vote || v == tag)) break;
    __builtin_amdgcn_s_sleep(2);
  }
  asm volatile("" ::: "memory");
}
DEVI void late_tag(u64& t) { asm volatile("" : "+v"(t)); t += 1; }
DEVI void lds_wait(LAS unsigned* p, unsigned want) {
#pragma unroll 1
  for (unsigned spins = 0; spins < (1u << 22); ++spins) {
    if (__builtin_amdgcn_readfirstlane(__hip_atomic_load(p, __ATOMIC_RELAXED, __HIP_MEMORY_SCOPE_WORKGROUP)) >= want) break;
    __builtin_amdgcn_s_sleep(1);
  }
  asm volatile("" ::: "memory");
}
DEVI unsigned pk_bf16(float lo, float hi) {
  b16x2 v = {(__bf16)lo, (__bf16)hi};
  return __builtin_bit_cast(unsigned, v);
}
DEVI float bf_lo(unsigned v) { return __builtin_bit_cast(float, v << 16); }
DEVI float bf_hi(unsigned v) { return __builtin_bit_cast(float, v & 0xffff0000u); }
DEVI float bf2f(u16 v) { return __builtin_bit_cast(float, (unsigned)v << 16); }
DEVI float rnd_bf(float x) { return bf_lo(pk_bf16(x, x) & 0xffffu); }
DEVI s16x8 lds_b128(LAS char* p) { return *(LAS s16x8*)p; }
DEVI s16x4 lds_tr(LAS char* p) { return __builtin_amdgcn_ds_read_tr16_b64_v4i16((LAS s16x4*)p); }
DEVI s16x8 cat4(s16x4 a, s16x4 b) { return __builtin_shufflevector(a, b, 0, 1, 2, 3, 4, 5, 6, 7); }
#define WAIT_VM(n) asm volatile("s_waitcnt vmcnt(" #n ")" ::: "memory")
#define WAIT_LGKM0() asm volatile("s_waitcnt lgkmcnt(0)" ::: "memory")
#define BARRIER() do { asm volatile("" ::: "memory"); __builtin_amdgcn_s_barrier(); asm volatile("" ::: "memory"); } while (0)
#define SCHED_FENCE() __builtin_amdgcn_sched_barrier(0)
#define ASM_TR(dst, addr, off) asm volatile("ds_read_b64_tr_b16 %0, %1 offset:%2" : "=v"(dst) : "v"(addr), "i"(off))
#define ASM_B128(dst, addr, off) asm volatile("ds_read_b128 %0, %1 offset:%2" : "=v"(dst) : "v"(addr), "i"(off))
#define ASM_B64(dst, addr, off) asm volatile("ds_read_b64 %0, %1 offset:%2" : "=v"(dst) : "v"(addr), "i"(off))

constexpr int G1_X = 0, G1_XS = 16384, G1_W = 2 * G1_XS, G1_WS = 12288;
#define HINT_STEP -1
struct G1W { s16x4 lo[6], hi[6]; };
struct G1X { s16x8 v[4]; };
#define G1W_REGS(f) "+v"(f.lo[0]), "+v"(f.lo[1]), "+v"(f.lo[2]), "+v"(f.lo[3]), "+v"(f.lo[4]), "+v"(f.lo[5]), \
                    "+v"(f.hi[0]), "+v"(f.hi[1]), "+v"(f.hi[2]), "+v"(f.hi[3]), "+v"(f.hi[4]), "+v"(f.hi[5])
DEVI void g1_wait(G1W& f) { asm volatile("s_waitcnt lgkmcnt(0)" : G1W_REGS(f) :: "memory"); }
DEVI void g1_wait(G1W& f, G1X& x) {
  asm volatile("s_waitcnt lgkmcnt(0)" : G1W_REGS(f), "+v"(x.v[0]), "+v"(x.v[1]), "+v"(x.v[2]), "+v"(x.v[3]) :: "memory");
}
template <int KS, int K>
DEVI void g1_rdw(G1W& n, unsigned a) {
  constexpr int t = K >> 1, off = (t / 3) * 6144 + KS * 3072 + (t % 3) * 64 + (K & 1) * 768;
  if (K & 1) ASM_TR(n.hi[t], a, off); else ASM_TR(n.lo[t], a, off);
}
template <int I, int KS, bool RD, bool RX, int G0, int ND, class DmaFn>
DEVI void g1_half(f32x16 (&acc)[6], const G1W& c, s16x8 xv, G1W& n, unsigned aw, G1X& nx, const unsigned (&ax)[4], unsigned xs, DmaFn&& dma) {
  constexpr int ti = (I >> 1) + (I & 1) * 3;
  acc[ti] = mfma32(cat4(c.lo[ti], c.hi[ti]), xv, acc[ti]);
  SCHED_FENCE();
  if constexpr (RD && I < 4) { g1_rdw<KS, 3 * I>(n, aw); g1_rdw<KS, 3 * I + 1>(n, aw); g1_rdw<KS, 3 * I + 2>(n, aw); }
  if constexpr (RX && I < 4) ASM_B128(nx.v[I], ax[I] + xs, 0);
  if constexpr (I >= G0 && I < G0 + ND) dma(I - G0);
  SCHED_FENCE();
  if constexpr (I + 1 < 6) g1_half<I + 1, KS, RD, RX, G0, ND>(acc, c, xv, n, aw, nx, ax, xs, dma);
}

DEVI void ffn_in(const u16* x, const u16* wg, const u16* wu, char* ws, LAS char* sm, int b, int w, int lane, u64& tag) {
  const int xcd = b & 7, li = b >> 3;
  const int panel = 8 * (xcd & 1) + (li & 7), col = 4 * (xcd >> 1) + (li >> 3);
  const int row0 = panel * 128, n0 = col * 96;
  const int hh = lane >> 5, t31 = lane & 31;
  const rsrc_t rx = mk_rsrc(x, SL * SD * 2);
  const rsrc_t rw = mk_rsrc(w < 2 ? wg : wu, SD * SF * 2);

  unsigned xo[2], wo[3];
#pragma unroll
  for (int i = 0; i < 2; ++i) {
    const int row = 32 * w + 8 * i + (lane >> 3), ch = (lane & 7) ^ ((row >> 1) & 7);
    xo[i] = (row0 + row) * (SD * 2) + ch * 16;
  }
#pragma unroll
  for (int i = 0; i < 3; ++i) {
    const int pp = (3 * w + i) % 6, o = pp * 1024 + lane * 16, k = o / 192;
    wo[i] = (k * SF + n0) * 2 + (o - 192 * k);
  }
  auto xpiece = [&](int D, int i) {
    dma16(rx, sm + G1_X + (D & 1) * G1_XS + (4 * w + i) * 1024, xo[i & 1], D * 128 + (i >> 1) * (16 * SD * 2));
  };
  auto wpiece = [&](int s, int i) {
    dma16(rw, sm + G1_W + (s & 3) * G1_WS + (3 * w + i) * 1024, wo[i], s * (32 * SF * 2));
  };
  xpiece(0, 0); xpiece(0, 1); xpiece(0, 2); xpiece(0, 3);
  wpiece(0, 0); wpiece(0, 1); wpiece(0, 2);
  wpiece(1, 0); wpiece(1, 1); wpiece(1, 2);
  xpiece(1, 0); xpiece(1, 1);
  wpiece(2, 0); wpiece(2, 1); wpiece(2, 2);

  const unsigned sbase = (unsigned)(uintptr_t)sm;
  unsigned ax[4];
#pragma unroll
  for (int q = 0; q < 4; ++q) ax[q] = sbase + G1_X + (32 * w + t31) * 128 + (((2 * q + hh) ^ ((t31 >> 1) & 7)) << 4);
  const unsigned aw = sbase + G1_W + (8 * hh + ((lane & 15) >> 2)) * 192 + (((lane >> 4) & 1) * 16 + (lane & 3) * 4) * 2;
  f32x16 acc[6];
#pragma unroll
  for (int i = 0; i < 6; ++i)
#pragma unroll
    for (int j = 0; j < 16; ++j) acc[i][j] = 0.f;

  G1W P, Q;
  G1X x0, x1;
  WAIT_VM(8); BARRIER();
  STAMP(0, 1);
  g1_rdw<0, 0>(P, aw); g1_rdw<0, 1>(P, aw); g1_rdw<0, 2>(P, aw); g1_rdw<0, 3>(P, aw); g1_rdw<0, 4>(P, aw); g1_rdw<0, 5>(P, aw);
  g1_rdw<0, 6>(P, aw); g1_rdw<0, 7>(P, aw); g1_rdw<0, 8>(P, aw); g1_rdw<0, 9>(P, aw); g1_rdw<0, 10>(P, aw); g1_rdw<0, 11>(P, aw);
  ASM_B128(x0.v[0], ax[0], 0); ASM_B128(x0.v[1], ax[1], 0); ASM_B128(x0.v[2], ax[2], 0); ASM_B128(x0.v[3], ax[3], 0);
  x1 = G1X{};
  auto none = [&](int) {};
  auto req = [&](int E, int i) {
    if (i < 2) xpiece((E & 1) ? (E + 3) / 2 : (E + 2) / 2, (E & 1) ? i : 2 + i);
    else wpiece(E + 3, i - 2);
  };
  g1_wait(P, x0);
  SCHED_FENCE();
  g1_half<0, 1, true, false, 1, 5>(acc, P, x0.v[0], Q, aw, x1, ax, 0u, [&](int i_) { req(0, i_); });

#define G1_EPOCH(E, ODD, XC, XN, VM, ST)                                                                        \
  do {                                                                                                          \
    g1_wait(Q);                                                                                                 \
    WAIT_VM(VM);                                                                                                \
    BARRIER();                                                                                                  \
    SCHED_FENCE();                                                                                              \
    const unsigned aw_ = aw + ((E) & 3) * G1_WS;                                                                \
    g1_half<0, 0, true, !(ODD), 4, (ST) ? 2 : 0>(acc, Q, XC.v[(ODD) ? 1 : 3], P, aw_, XN, ax, (((E) >> 1) & 1) * G1_XS, \
                                                 [&](int i_) { req((E), i_); });                                \
    g1_wait(P, XN);                                                                                             \
    SCHED_FENCE();                                                                                              \
    g1_half<0, 1, true, false, 3, (ST) ? 3 : 0>(acc, P, XN.v[(ODD) ? 2 : 0], Q, aw_, XN, ax, 0u,                \
                                                [&](int i_) { req((E), 2 + i_); });                             \
  } while (0)

#pragma unroll 1
  for (int e = 1; e < 17; e += 4) {
    G1_EPOCH(e, true, x0, x0, 10, true);
    G1_EPOCH(e + 1, false, x0, x1, 8, true);
    G1_EPOCH(e + 2, true, x1, x1, 10, true);
    G1_EPOCH(e + 3, false, x1, x0, 8, true);
  }
  G1_EPOCH(17, true, x0, x0, 10, false);
  G1_EPOCH(18, false, x0, x1, 3, false);
  G1_EPOCH(19, true, x1, x1, 0, false);
  g1_wait(Q);
  SCHED_FENCE();
  g1_half<0, 0, false, false, 0, 0>(acc, Q, x1.v[3], P, aw, x1, ax, 0u, none);
#undef G1_EPOCH
  STAMP(0, 2);
  __builtin_amdgcn_s_setprio(PRIO_EPI);

  const rsrc_t rh = mk_rsrc(ws + OFF_HF, SZ_HF);
  const unsigned hbase = ((((row0 + 32 * w) >> 4) + (t31 >> 4)) * 48 + 3 * col) * 1024 + (hh * 16 + (t31 & 15)) * 16;
#pragma unroll
  for (int c = 0; c < 3; ++c)
#pragma unroll
    for (int sq = 0; sq < 2; ++sq) {
      float r[8];
#pragma unroll
      for (int j = 0; j < 8; ++j) {
        const float gv = acc[c][8 * sq + j], uv = acc[3 + c][8 * sq + j];
        const float e = __builtin_amdgcn_exp2f(gv * -1.4426950408889634f);
        r[j] = gv * uv * __builtin_amdgcn_rcpf(1.0f + e);
      }
      bstore16_sc1(u32x4{pk_bf16(r[0], r[1]), pk_bf16(r[2], r[3]), pk_bf16(r[4], r[5]), pk_bf16(r[6], r[7])}, rh,
                   hbase + c * 1024 + sq * 512);
    }
  STAMP(0, 3);
  WAIT_VM(0);
  late_tag(tag);
  BARRIER();
  if (w == 0 && lane == 0) flag_store((gu64*)(ws + OFF_FLAG) + FL_H + panel * 16 + col, tag);
  STAMP(0, 4);
}

constexpr int F_RED = 0;
constexpr int F_AT = 61440;
constexpr int F_OUT = 71680;

DEVI void ffn_out(u16* out, char* ws, LAS char* sm, int b, int w, int lane, u64 tag) {
  const int xcd = b & 7, li = b >> 3;
  const int rt = 8 * (xcd & 3) + (li & 7), cg = 4 * (xcd >> 2) + (li >> 3);
  const int g = lane >> 4, l15 = lane & 15;
  gu64* fl = (gu64*)(ws + OFF_FLAG);
  const rsrc_t rh = mk_rsrc(ws + OFF_HF, SZ_HF);
  const rsrc_t rf = mk_rsrc(ws + OFF_WDF, SZ_WDF);
  const unsigned ho = (4 * rt * 48 + 12 * w) * 1024 + lane * 16;
  const unsigned wo = (5 * cg * 48 + 12 * w) * 1024 + lane * 16;
  f32x4 acc[4][5];
#pragma unroll
  for (int tt = 0; tt < 4; ++tt)
#pragma unroll
    for (int dd = 0; dd < 5; ++dd) acc[tt][dd] = f32x4{0.f, 0.f, 0.f, 0.f};
  u32x4 hb[3][4], wb[3][5];
  flags_wait(fl + FL_M + cg * 32 + 6 * w + (lane < 6 ? lane : 0), lane < 6, tag);
#pragma unroll
  for (int s = 0; s < 3; ++s)
#pragma unroll
    for (int dd = 0; dd < 5; ++dd) wb[s][dd] = bload16_sc1(rf, wo, dd * 48 * 1024 + s * 1024);
  flags_wait(fl + FL_H + (rt >> 1) * 16 + 4 * w + (lane & 3), true, tag);
  STAMP(0, 5);
#pragma unroll
  for (int s = 0; s < 3; ++s)
#pragma unroll
    for (int tt = 0; tt < 4; ++tt) hb[s][tt] = bload16_sc1(rh, ho, tt * 48 * 1024 + s * 1024);
  SCHED_FENCE();
#pragma unroll
  for (int s = 0; s < 12; ++s) {
    const int slot = s % 3;
#pragma unroll
    for (int dd = 0; dd < 5; ++dd) {
#pragma unroll
      for (int tt = 0; tt < 4; ++tt) {
        acc[tt][dd] = mfma16u(wb[slot][dd], hb[slot][tt], acc[tt][dd]);
        SCHED_FENCE();
        if (s + 3 < 12) {
          if (tt == 3) wb[slot][dd] = bload16_sc1(rf, wo, dd * 48 * 1024 + (s + 3) * 1024);
          if (dd == 4) hb[slot][tt] = bload16_sc1(rh, ho, tt * 48 * 1024 + (s + 3) * 1024);
        }
        SCHED_FENCE();
      }
    }
  }
  STAMP(0, 6);
  LAS char* red = sm + F_RED;
#pragma unroll
  for (int tt = 0; tt < 4; ++tt) {
    if (tt != w) {
      const int slot = (tt * 3 + (w > tt ? w - 1 : w)) * 5;
#pragma unroll
      for (int dd = 0; dd < 5; ++dd) *(LAS f32x4*)(red + (slot + dd) * 1024 + lane * 16) = acc[tt][dd];
    }
  }
  flags_wait(lane < 32 ? fl + FL_A + cg * 32 + lane : fl + FL_M + (lane & 7) * 32 + rt, lane < 40, tag);
  unsigned gtb;
  {
    const rsrc_t ra = mk_rsrc(ws + OFF_AT, SZ_AT);
    u32x4 av[3];
#pragma unroll
    for (int k = 0; k < 3; ++k)
      av[k] = bload16_sc1(ra, k < 2 || w < 2 ? (rt * SD + 80 * cg) * 128 + (256 * k + 64 * w + lane) * 16 : 0xfffffff0u, 0);
    gtb = __builtin_amdgcn_raw_buffer_load_b32(mk_rsrc(ws + OFF_GATE, SZ_GATE), (64 * rt + 16 * w + l15) * 4, 0, 16);
    asm volatile("" : "+v"(av[0]), "+v"(av[1]), "+v"(av[2]), "+v"(gtb));
#pragma unroll
    for (int k = 0; k < 3; ++k)
      if (k < 2 || w < 2) *(LAS u32x4*)(sm + F_AT + (256 * k + 64 * w + lane) * 16) = av[k];
  }
  const float gt = __builtin_bit_cast(float, gtb);
  WAIT_LGKM0();
  BARRIER();
  STAMP(0, 7);
  f32x4 bs[5];
#pragma unroll
  for (int dd = 0; dd < 5; ++dd) {
    f32x4 v = w == 0 ? acc[0][dd] : w == 1 ? acc[1][dd] : w == 2 ? acc[2][dd] : acc[3][dd];
#pragma unroll
    for (int k = 0; k < 3; ++k) v += *(LAS f32x4*)(red + ((w * 3 + k) * 5 + dd) * 1024 + lane * 16);
    bs[dd] = v;
  }
  const float og = rnd_bf(1.0f - gt);
  LAS char* os = sm + F_OUT + w * 2560;
#pragma unroll
  for (int dd = 0; dd < 5; ++dd) {
    const s16x4 av = lds_tr(sm + F_AT + (16 * dd + 4 * g + (l15 >> 2)) * 128 + (16 * w + 4 * (lane & 3)) * 2);
    float r[4];
#pragma unroll
    for (int e = 0; e < 4; ++e) r[e] = fmaf(gt, bf2f((u16)av[e]), og * bs[dd][e]);
    *(LAS u32x2*)(os + l15 * 160 + dd * 32 + g * 8) = u32x2{pk_bf16(r[0], r[1]), pk_bf16(r[2], r[3])};
  }
  WAIT_LGKM0();
  const rsrc_t ro = mk_rsrc(out, SL * SD * 2);
#pragma unroll
  for (int k = 0; k < 3; ++k) {
    const int c = 64 * k + lane;
    if (c < 160) {
      const int row = c / 10, ch = c - 10 * row;
      const u32x4 v = *(LAS u32x4*)(os + c * 16);
      __builtin_amdgcn_raw_buffer_store_b128(v, ro, ((64 * rt + 16 * w + row) * SD + 80 * cg) * 2 + ch * 16, 0, 2);
    }
  }
  STAMP(0, 8);
}

constexpr int CP_STRIDE = 4416;
constexpr int CP_CH = 4 * CP_STRIDE;
constexpr int U_BLK = 160, U_CH = 33 * U_BLK;
constexpr int C_CP = 0;
constexpr int C_U = C_CP + 3 * CP_CH;
constexpr int C_ST = C_U + 3 * U_CH;
constexpr int C_SYNC = C_ST + 4 * 2048;
constexpr int C_MISC = C_U;

DEVI f32x2 csq(f32x2 a) {
  float t0, t1, r0, r1;
  asm volatile("v_mul_f32 %0, %1, %1" : "=v"(t0) : "v"(a.y));
  asm volatile("v_mul_f32 %0, %1, %2" : "=v"(t1) : "v"(a.x), "v"(a.y));
  asm volatile("v_fma_f32 %0, %1, %1, -%2" : "=v"(r0) : "v"(a.x), "v"(t0));
  asm volatile("v_add_f32 %0, %1, %1" : "=v"(r1) : "v"(t1));
  return f32x2{r0, r1};
}
DEVI unsigned hmul_a(unsigned a, unsigned b) {
  unsigned t;
  asm volatile("v_pk_mul_f16 %0, %1, %2 op_sel:[0,0] op_sel_hi:[0,1]" : "=v"(t) : "v"(a), "v"(b));
  return t;
}
DEVI unsigned hmul_b(unsigned a, unsigned b, unsigned t) {
  unsigned r;
  asm volatile("v_pk_fma_f16 %0, %1, %2, %3 op_sel:[1,1,0] op_sel_hi:[1,0,1] neg_lo:[0,1,0]" : "=v"(r) : "v"(a), "v"(b), "v"(t));
  return r;
}
DEVI unsigned pk_f16(float lo, float hi) {
  f16x2 v = {(_Float16)lo, (_Float16)hi};
  return __builtin_bit_cast(unsigned, v);
}
DEVI float exp_f32(float x) {
  const float t = x * 1.4426950408889634f;
  const float lo = fmaf(x, 1.4426950408889634f, -t) + x * 1.9259629911266175e-8f;
  const float e = __builtin_amdgcn_exp2f(t);
  return fmaf(e, lo * 0.6931471805599453f, e);
}
DEVI f32x2 cexp_pos(float zr2, float zi2, float p) {
  const float e = __builtin_amdgcn_exp2f(zr2 * p);
  const float th = __builtin_amdgcn_fractf(zi2 * p);
  return f32x2{e * __builtin_amdgcn_cosf(th), e * __builtin_amdgcn_sinf(th)};
}
constexpr int KG_ROW = 288;

struct KgOps { s16x8 x[3][4], y[4]; };
DEVI void kg_read(KgOps& o, unsigned base) {
#define KGR(s)                                                                              \
  ASM_B128(o.y[s], base, 41 * KG_ROW + 64 * s); ASM_B128(o.x[0][s], base, 64 * s);          \
  ASM_B128(o.x[1][s], base, 16 * KG_ROW + 64 * s); ASM_B128(o.x[2][s], base, 32 * KG_ROW + 64 * s);
  KGR(0) KGR(1) KGR(2) KGR(3)
#undef KGR
}
DEVI void kg_wait(KgOps& o) {
  asm volatile("s_waitcnt lgkmcnt(0)"
               : "+v"(o.y[0]), "+v"(o.y[1]), "+v"(o.y[2]), "+v"(o.y[3]), "+v"(o.x[0][0]), "+v"(o.x[0][1]), "+v"(o.x[0][2]), "+v"(o.x[0][3]),
                 "+v"(o.x[1][0]), "+v"(o.x[1][1]), "+v"(o.x[1][2]), "+v"(o.x[1][3]), "+v"(o.x[2][0]), "+v"(o.x[2][1]), "+v"(o.x[2][2]), "+v"(o.x[2][3])
               :: "memory");
}
DEVI void kg_mma(f32x4 (&acc)[3], const KgOps& o) {
#pragma unroll
  for (int s = 0; s < 4; ++s)
#pragma unroll
    for (int i = 0; i < 3; ++i)
      acc[i] = __builtin_amdgcn_mfma_f32_16x16x32_f16(__builtin_bit_cast(f16x8, o.x[i][s]), __builtin_bit_cast(f16x8, o.y[s]), acc[i], 0, 0, 0);
}

DEVI unsigned touch_line(const u16* x, const u16* wg, const u16* wu, int i, int w, int lane) {
  const int n = (w * 256 + i) * 64 + lane;
  const rsrc_t rx = mk_rsrc(x, SL * SD * 2), rg = mk_rsrc(wg, SD * SF * 2), ru = mk_rsrc(wu, SD * SF * 2);
  const unsigned off = 0xfffffff0u;
  const unsigned a = __builtin_amdgcn_raw_buffer_load_b32(rg, n < 15360 ? n * 128u : off, 0, 0);
  const unsigned b = __builtin_amdgcn_raw_buffer_load_b32(ru, n >= 15360 && n < 30720 ? (n - 15360) * 128u : off, 0, 0);
  const unsigned c = __builtin_amdgcn_raw_buffer_load_b32(rx, n >= 30720 && n < 51200 ? (n - 30720) * 128u : off, 0, 0);
  return a ^ b ^ c;
}

DEVI void kgen_wave(const u16* logA, const u16* Aim, const u16* Cre, const u16* Cim, const u16* logdt,
                    LAS char* sm, LAS char* kqp, LAS unsigned* misc_free, int d, int lane,
                    const u16* x, const u16* wg, const u16* wu, int i, int w, char* ws, u64 tag) {
  const int g = lane >> 4, l15 = lane & 15;
  u16 pla[3], pai[3], pcr[3], pci[3];
#pragma unroll
  for (int c = 0; c < 3; ++c) {
    const int idx = d * SN + 64 * c + lane;
    pla[c] = logA[idx]; pai[c] = Aim[idx]; pcr[c] = Cre[idx]; pci[c] = Cim[idx];
  }
  const u16 ldt = logdt[d];
  const unsigned tch = touch_line(x, wg, wu, i, w, lane);
  __builtin_amdgcn_s_setprio(PRIO_KGEN);
  const float dt = rnd_bf(exp_f32(bf2f(ldt)));
  unsigned cm = 0;
#pragma unroll
  for (int c = 0; c < 3; ++c) cm = max(cm, max((unsigned)(pcr[c] & 0x7fff), (unsigned)(pci[c] & 0x7fff)));
#pragma unroll
  for (int o = 1; o < 64; o <<= 1) cm = max(cm, (unsigned)__shfl_xor((int)cm, o));
  const unsigned ce = (__builtin_bit_cast(unsigned, bf2f((u16)cm) * dt) >> 23) & 0xffu;
  const bool cok = ce >= 1 && ce <= 253;
  const float cs = cok ? __builtin_bit_cast(float, (254u - ce) << 23) : 1.f, cinv2 = 2.f * (cok ? __builtin_bit_cast(float, ce << 23) : 1.f);
  f32x2 aC[3], a1[3], a128[3];
#pragma unroll
  for (int c = 0; c < 3; ++c) {
    const float zr = -rnd_bf(exp_f32(bf2f(pla[c]))) * dt, zi = bf2f(pai[c]) * dt;
    const float zr2 = zr * 1.4426950408889634f, zi2 = zi * 0.15915494309189535f;
    aC[c] = f32x2{bf2f(pcr[c]) * dt * cs, bf2f(pci[c]) * dt * cs};
    a1[c] = cexp_pos(zr2, zi2, 1.f);
    a128[c] = cexp_pos(zr2, zi2, 128.f);
  }
  f32x4 acc[3] = {f32x4{0.f, 0.f, 0.f, 0.f}, f32x4{0.f, 0.f, 0.f, 0.f}, f32x4{0.f, 0.f, 0.f, 0.f}};
  LAS char* tw = sm + lane * 4;
  const unsigned rbase = (unsigned)(uintptr_t)sm + l15 * KG_ROW + 16 * g;
  auto put = [&](int row, unsigned v) { *(LAS unsigned*)(tw + row * KG_ROW) = v; };
  KgOps ops;
#pragma unroll 1
  for (int c = 0; c < 3; ++c) {
    const f32x2 C = aC[0], w1 = a1[0], w128 = a128[0];
    aC[0] = aC[1]; a1[0] = a1[1]; a128[0] = a128[1];
    aC[1] = aC[2]; a1[1] = a1[2]; a128[1] = a128[2];
    asm volatile("s_nop 1" ::: "memory");
    const f32x2 w2 = csq(w1);
    const f32x2 w256 = csq(w128);
    const f32x2 w4 = csq(w2);
    const f32x2 w512 = csq(w256);
    const f32x2 y1 = csq(w4);
    const f32x2 w1024 = csq(w512);
    const f32x2 y2 = csq(y1);
    const f32x2 y4 = csq(y2);
    const f32x2 y8 = csq(y4);
    if (c) { kg_wait(ops); kg_mma(acc, ops); }
    SCHED_FENCE();
    const unsigned hw1 = pk_f16(w1.x, w1.y);
    const unsigned hw2 = pk_f16(w2.x, w2.y);
    const unsigned hw4 = pk_f16(w4.x, w4.y);
    const unsigned hw128 = pk_f16(w128.x, w128.y);
    const unsigned hw256 = pk_f16(w256.x, w256.y);
    const unsigned hw512 = pk_f16(w512.x, w512.y);
    const unsigned hw1024 = pk_f16(w1024.x, w1024.y);
    const unsigned hy1 = pk_f16(y1.x, -y1.y);
    const unsigned hy2 = pk_f16(y2.x, -y2.y);
    const unsigned hy4 = pk_f16(y4.x, -y4.y);
    const unsigned hy8 = pk_f16(y8.x, -y8.y);
    const unsigned c0 = pk_f16(C.x, C.y);
    const unsigned t_c1 = hmul_a(c0, hw1);
    const unsigned t_c2 = hmul_a(c0, hw2);
    const unsigned t_c4 = hmul_a(c0, hw4);
    SCHED_FENCE();
    put(42, hy1); put(43, hy2); SCHED_FENCE();
    const unsigned t_b0 = hmul_a(c0, hw256);
    const unsigned t_e0 = hmul_a(c0, hw512);
    const unsigned t_d0 = hmul_a(c0, hw1024);
    SCHED_FENCE();
    const unsigned t_g0 = hmul_a(c0, hw128);
    const unsigned t_hy3 = hmul_a(hy1, hy2);
    const unsigned t_hy5 = hmul_a(hy1, hy4);
    SCHED_FENCE();
    const unsigned t_hy6 = hmul_a(hy2, hy4);
    const unsigned c1 = hmul_b(c0, hw1, t_c1);
    const unsigned c2 = hmul_b(c0, hw2, t_c2);
    SCHED_FENCE();
    const unsigned c4 = hmul_b(c0, hw4, t_c4);
    const unsigned b0 = hmul_b(c0, hw256, t_b0);
    const unsigned e0 = hmul_b(c0, hw512, t_e0);
    SCHED_FENCE();
    const unsigned d0 = hmul_b(c0, hw1024, t_d0);
    const unsigned g0 = hmul_b(c0, hw128, t_g0);
    const unsigned hy3 = hmul_b(hy1, hy2, t_hy3);
    SCHED_FENCE();
    const unsigned hy5 = hmul_b(hy1, hy4, t_hy5);
    const unsigned hy6 = hmul_b(hy2, hy4, t_hy6);
    const unsigned t_c3 = hmul_a(c1, hw2);
    const unsigned t_c5 = hmul_a(c1, hw4);
    const unsigned t_c6 = hmul_a(c2, hw4);
    SCHED_FENCE();
    put(0, c0); put(1, c1); SCHED_FENCE();
    const unsigned t_b1 = hmul_a(b0, hw2);
    const unsigned t_b2 = hmul_a(b0, hw4);
    const unsigned t_f0 = hmul_a(e0, hw4);
    SCHED_FENCE();
    put(44, hy3); put(45, hy4); SCHED_FENCE();
    const unsigned t_d1 = hmul_a(d0, hw128);
    const unsigned t_d2 = hmul_a(d0, hw256);
    const unsigned t_d4 = hmul_a(d0, hw512);
    SCHED_FENCE();
    put(46, hy5); put(47, hy6); SCHED_FENCE();
    const unsigned t_d8 = hmul_a(d0, hw1024);
    const unsigned t_hy7 = hmul_a(hy3, hy4);
    const unsigned t_hy9 = hmul_a(hy1, hy8);
    SCHED_FENCE();
    const unsigned t_hy10 = hmul_a(hy2, hy8);
    const unsigned t_hy11 = hmul_a(hy3, hy8);
    const unsigned t_hy12 = hmul_a(hy4, hy8);
    SCHED_FENCE();
    const unsigned t_hy13 = hmul_a(hy5, hy8);
    const unsigned t_hy14 = hmul_a(hy6, hy8);
    const unsigned t_g1 = hmul_a(c1, hw128);
    SCHED_FENCE();
    const unsigned t_g2 = hmul_a(c2, hw128);
    const unsigned t_g4 = hmul_a(c4, hw128);
    const unsigned t_p0 = hmul_a(b0, hw128);
    SCHED_FENCE();
    const unsigned t_e1 = hmul_a(e0, hw128);
    const unsigned t_e2 = hmul_a(e0, hw256);
    const unsigned c3 = hmul_b(c1, hw2, t_c3);
    SCHED_FENCE();
    const unsigned c5 = hmul_b(c1, hw4, t_c5);
    const unsigned c6 = hmul_b(c2, hw4, t_c6);
    const unsigned b1 = hmul_b(b0, hw2, t_b1);
    SCHED_FENCE();
    const unsigned b2 = hmul_b(b0, hw4, t_b2);
    const unsigned f0 = hmul_b(e0, hw4, t_f0);
    const unsigned d1 = hmul_b(d0, hw128, t_d1);
    SCHED_FENCE();
    const unsigned d2 = hmul_b(d0, hw256, t_d2);
    const unsigned d4 = hmul_b(d0, hw512, t_d4);
    const unsigned d8 = hmul_b(d0, hw1024, t_d8);
    SCHED_FENCE();
    const unsigned hy7 = hmul_b(hy3, hy4, t_hy7);
    const unsigned hy9 = hmul_b(hy1, hy8, t_hy9);
    const unsigned hy10 = hmul_b(hy2, hy8, t_hy10);
    SCHED_FENCE();
    const unsigned hy11 = hmul_b(hy3, hy8, t_hy11);
    const unsigned hy12 = hmul_b(hy4, hy8, t_hy12);
    const unsigned hy13 = hmul_b(hy5, hy8, t_hy13);
    SCHED_FENCE();
    const unsigned hy14 = hmul_b(hy6, hy8, t_hy14);
    const unsigned g1 = hmul_b(c1, hw128, t_g1);
    const unsigned g2 = hmul_b(c2, hw128, t_g2);
    SCHED_FENCE();
    const unsigned g4 = hmul_b(c4, hw128, t_g4);
    const unsigned p0 = hmul_b(b0, hw128, t_p0);
    const unsigned e1 = hmul_b(e0, hw128, t_e1);
    SCHED_FENCE();
    const unsigned e2 = hmul_b(e0, hw256, t_e2);
    const unsigned t_c7 = hmul_a(c3, hw4);
    const unsigned t_b3 = hmul_a(b1, hw4);
    const unsigned t_g3 = hmul_a(c3, hw128);
    SCHED_FENCE();
    put(2, c2); put(3, c3); SCHED_FENCE();
    const unsigned t_g5 = hmul_a(c5, hw128);
    const unsigned t_g6 = hmul_a(c6, hw128);
    const unsigned t_p1 = hmul_a(b1, hw128);
    SCHED_FENCE();
    put(4, c4); put(5, c5); SCHED_FENCE();
    const unsigned t_p2 = hmul_a(b2, hw128);
    const unsigned t_f1 = hmul_a(f0, hw128);
    const unsigned t_f2 = hmul_a(f0, hw256);
    SCHED_FENCE();
    put(8, g0); put(9, g1); SCHED_FENCE();
    const unsigned t_e3 = hmul_a(e1, hw256);
    const unsigned t_d3 = hmul_a(d1, hw256);
    const unsigned t_d5 = hmul_a(d1, hw512);
    SCHED_FENCE();
    put(16, b0); put(17, b1); SCHED_FENCE();
    const unsigned t_d6 = hmul_a(d2, hw512);
    const unsigned t_hy15 = hmul_a(hy7, hy8);
    const unsigned c7 = hmul_b(c3, hw4, t_c7);
    SCHED_FENCE();
    put(24, e0); put(25, f0); SCHED_FENCE();
    const unsigned b3 = hmul_b(b1, hw4, t_b3);
    const unsigned g3 = hmul_b(c3, hw128, t_g3);
    const unsigned g5 = hmul_b(c5, hw128, t_g5);
    SCHED_FENCE();
    put(32, d0); put(33, d1); SCHED_FENCE();
    const unsigned g6 = hmul_b(c6, hw128, t_g6);
    const unsigned p1 = hmul_b(b1, hw128, t_p1);
    const unsigned p2 = hmul_b(b2, hw128, t_p2);
    SCHED_FENCE();
    put(40, d8); put(41, 0x3c00u); SCHED_FENCE();
    const unsigned f1 = hmul_b(f0, hw128, t_f1);
    const unsigned f2 = hmul_b(f0, hw256, t_f2);
    const unsigned e3 = hmul_b(e1, hw256, t_e3);
    SCHED_FENCE();
    put(48, hy7); put(49, hy8); SCHED_FENCE();
    const unsigned d3 = hmul_b(d1, hw256, t_d3);
    const unsigned d5 = hmul_b(d1, hw512, t_d5);
    const unsigned d6 = hmul_b(d2, hw512, t_d6);
    SCHED_FENCE();
    put(50, hy9); put(51, hy10); SCHED_FENCE();
    const unsigned hy15 = hmul_b(hy7, hy8, t_hy15);
    const unsigned t_g7 = hmul_a(c7, hw128);
    const unsigned t_p3 = hmul_a(b3, hw128);
    const unsigned t_f3 = hmul_a(f1, hw256);
    SCHED_FENCE();
    put(6, c6); put(7, c7); SCHED_FENCE();
    const unsigned t_d7 = hmul_a(d3, hw512);
    const unsigned g7 = hmul_b(c7, hw128, t_g7);
    const unsigned p3 = hmul_b(b3, hw128, t_p3);
    SCHED_FENCE();
    put(10, g2); put(11, g3); SCHED_FENCE();
    const unsigned f3 = hmul_b(f1, hw256, t_f3);
    const unsigned d7 = hmul_b(d3, hw512, t_d7);
    put(12, g4); put(13, g5); SCHED_FENCE();
    put(14, g6); put(15, g7); SCHED_FENCE();
    put(18, b2); put(19, b3); SCHED_FENCE();
    put(20, p0); put(21, p1); SCHED_FENCE();
    put(22, p2); put(23, p3); SCHED_FENCE();
    put(26, e1); put(27, f1); SCHED_FENCE();
    put(28, e2); put(29, f2); SCHED_FENCE();
    put(30, e3); put(31, f3); SCHED_FENCE();
    put(34, d2); put(35, d3); SCHED_FENCE();
    put(36, d4); put(37, d5); SCHED_FENCE();
    put(38, d6); put(39, d7); SCHED_FENCE();
    put(52, hy11); put(53, hy12); SCHED_FENCE();
    put(54, hy13); put(55, hy14); SCHED_FENCE();
    put(56, hy15);
    WAIT_LGKM0();
    kg_read(ops, rbase);
  }
  kg_wait(ops); kg_mma(acc, ops);
  asm volatile("" :: "v"(tch));
  lds_wait(misc_free, 1u);
  LAS float* kq = (LAS float*)kqp;
#pragma unroll
  for (int e = 0; e < 4; ++e) {
    const int r = 4 * g + e;
    kq[(r & 7) + 128 * (r >> 3) + 8 * l15] = cinv2 * acc[0][e];
    const int rr = r - 8;
    const int i1 = r < 8 ? 256 + (r & 3) + 64 * (r >> 2) + 4 * l15 : 384 + (rr & 1) + 32 * (rr >> 1) + 2 * l15;
    kq[i1] = cinv2 * acc[1][e];
    if (r < 8) kq[512 + 16 * r + l15] = cinv2 * acc[2][e];
    else if (r == 8 && l15 == 0) kq[640] = cinv2 * acc[2][e];
  }
  WAIT_LGKM0();
  u32x4 kv[5];
  {
    const float a0 = kq[639 - lane], b0 = kq[640 - lane];
    const float a1 = kq[575 - lane], b1 = kq[576 - lane];
    const float a2 = kq[510 - 2 * lane], b2 = kq[511 - 2 * lane], c2 = kq[512 - 2 * lane];
    const int base = lane < 32 ? 380 - 4 * lane : 504 - 8 * lane;
    const f32x4 e0 = *(LAS f32x4*)(kq + base), e1 = *(LAS f32x4*)(kq + base + 4);
    const unsigned pa0 = pk_bf16(a0, a0), pb0 = pk_bf16(b0, b0), pa1 = pk_bf16(a1, a1), pb1 = pk_bf16(b1, b1);
    kv[0] = u32x4{pb0, (lane & 1) ? pk_bf16(b0, a0) : pb0, pa0, pa0};
    kv[1] = u32x4{pb1, (lane & 1) ? pk_bf16(b1, a1) : pb1, pa1, pa1};
    kv[2] = u32x4{pk_bf16(c2, c2), pk_bf16(b2, b2), pk_bf16(b2, a2), pk_bf16(a2, a2)};
    const bool lo = lane < 32;
    kv[3] = u32x4{lo ? pk_bf16(e1[0], e0[3]) : pk_bf16(e1[3], e1[2]), lo ? pk_bf16(e0[2], e0[2]) : pk_bf16(e1[1], e1[0]),
                  lo ? pk_bf16(e0[2], e0[1]) : pk_bf16(e0[3], e0[2]), lo ? pk_bf16(e0[0], e0[0]) : pk_bf16(e0[1], e0[0])};
    kv[4] = u32x4{0, 0, 0, 0};
  }
#pragma unroll
  for (int k = 0; k < 4; ++k) *(LAS u32x4*)(sm + (64 * k + lane) * 16) = kv[k];
  if (lane < 9) *(LAS u32x4*)(sm + (256 + lane) * 16) = kv[4];
  WAIT_LGKM0();
  u32x2 nx[5];
#pragma unroll
  for (int k = 0; k < 5; ++k) nx[k] = *(LAS u32x2*)(sm + (k < 4 || lane < 8 ? 64 * k + lane + 1 : 256) * 16);
#pragma unroll
  for (int k = 0; k < 5; ++k) {
    const int m = 64 * k + lane;
    if (m < 264) {
      const u32x4 a = kv[k];
      const u32x2 n = nx[k];
      *(LAS u32x4*)(sm + CP_STRIDE + m * 16) =
          u32x4{__builtin_amdgcn_alignbyte(a.y, a.x, 2), __builtin_amdgcn_alignbyte(a.z, a.y, 2),
                __builtin_amdgcn_alignbyte(a.w, a.z, 2), __builtin_amdgcn_alignbyte(n.x, a.w, 2)};
      *(LAS u32x4*)(sm + 2 * CP_STRIDE + m * 16) = u32x4{a.y, a.z, a.w, n.x};
      *(LAS u32x4*)(sm + 3 * CP_STRIDE + m * 16) =
          u32x4{__builtin_amdgcn_alignbyte(a.z, a.y, 2), __builtin_amdgcn_alignbyte(a.w, a.z, 2),
                __builtin_amdgcn_alignbyte(n.x, a.w, 2), __builtin_amdgcn_alignbyte(n.y, n.x, 2)};
    }
  }
  WAIT_LGKM0();
}

DEVI void u_fill(char* ws, LAS char* ub, int d, int cg, int lane, u64 tag) {
  flags_wait((gu64*)(ws + OFF_FLAG) + FL_M + cg * 32 + (lane & 31), true, tag);
  const rsrc_t rut = mk_rsrc(ws + OFF_UT, SZ_UT);
  u32x4 uv[4];
#pragma unroll
  for (int k = 0; k < 4; ++k) uv[k] = bload16_sc1(rut, d * (SL * 2) + (64 * k + lane) * 16, 0);
  if (lane < 10) *(LAS u32x4*)(ub + lane * 16) = u32x4{0, 0, 0, 0};
#pragma unroll
  for (int k = 0; k < 4; ++k) {
    const int m = 64 * k + lane;
    *(LAS u32x4*)(ub + (1 + (m >> 3)) * U_BLK + (m & 7) * 16) = uv[k];
  }
  WAIT_LGKM0();
}

DEVI void misc_wave(const u16* u, const u16* wg, const u16* wu, const u16* wd, const u16* gw, char* ws, LAS char* sm,
                    LAS unsigned* misc_free, int i, int lane, u64& tag) {
  const int ii = i;
  const int g = lane >> 4, l15 = lane & 15, q = l15 >> 2, p = lane & 3;
  const rsrc_t ru = mk_rsrc(u, SL * SD * 2);
  const rsrc_t rwd = mk_rsrc(wd, SF * SD * 2);
  const int rt = i >> 3, cg = i & 7;
  const unsigned tch = touch_line(u, wg, wu, ii, 3, lane);
#pragma unroll
  for (int k = 0; k < 10; ++k) {
    const int c = 64 * k + lane, row = c / 10, ch = c - 10 * row;
    dma16(ru, sm + k * 1024, ((64 * rt + row) * SD + 80 * cg) * 2 + ch * 16, 0);
  }
  if (rt < 24) {
#pragma unroll
    for (int k = 0; k < 10; ++k) {
      const int c = 64 * k + lane, row = c / 10, ch = c - 10 * row;
      dma16(rwd, sm + 10240 + k * 1024, ((64 * rt + row) * SD + 80 * cg) * 2 + ch * 16, 0);
    }
  }
  {
    const int row = 8 * i + (lane >> 3), seg = lane & 7;
    const rsrc_t rg = mk_rsrc(gw, SD * 2);
    float s = 0.f;
#pragma unroll
    for (int k = 0; k < 10; ++k) {
      const u32x4 xv = bload16(ru, row * (SD * 2) + (seg + 8 * k) * 16, 0);
      const u32x4 gv = bload16(rg, (seg + 8 * k) * 16, 0);
#pragma unroll
      for (int j = 0; j < 4; ++j) {
        s = fmaf(bf_lo(xv[j]), bf_lo(gv[j]), s);
        s = fmaf(bf_hi(xv[j]), bf_hi(gv[j]), s);
      }
    }
    s += __shfl_xor(s, 1); s += __shfl_xor(s, 2); s += __shfl_xor(s, 4);
    s = rnd_bf(s);
    const float gt = rnd_bf(1.0f / (1.0f + expf(-s)));
    if (seg == 0)
      __hip_atomic_store((GAS unsigned*)(ws + OFF_GATE) + row, __builtin_bit_cast(unsigned, gt), __ATOMIC_RELAXED, __HIP_MEMORY_SCOPE_AGENT);
  }
  WAIT_VM(0);
  asm volatile("" :: "v"(tch));
  const rsrc_t rut = mk_rsrc(ws + OFF_UT, SZ_UT);
#pragma unroll
  for (int c = 0; c < 5; ++c)
#pragma unroll
    for (int h = 0; h < 2; ++h) {
      const int t0 = 32 * h + 8 * g;
      LAS char* a = sm + (t0 + q) * 160 + c * 32 + p * 8;
      const s16x8 v = cat4(lds_tr(a), lds_tr(a + 4 * 160));
      bstore16_sc1(__builtin_bit_cast(u32x4, v), rut, ((80 * cg + 16 * c + l15) * SL + 64 * rt + t0) * 2);
    }
  if (rt < 24) {
    const rsrc_t rf = mk_rsrc(ws + OFF_WDF, SZ_WDF);
#pragma unroll
    for (int fb = 0; fb < 2; ++fb)
#pragma unroll
      for (int c = 0; c < 5; ++c) {
        LAS char* a = sm + 10240 + (32 * fb + 16 * (g >> 1) + 4 * (g & 1) + q) * 160 + c * 32 + p * 8;
        const s16x8 v = cat4(lds_tr(a), lds_tr(a + 8 * 160));
        bstore16_sc1(__builtin_bit_cast(u32x4, v), rf, ((5 * cg + c) * 48 + 2 * rt + fb) * 1024 + lane * 16);
      }
  }
  WAIT_LGKM0();
  if (lane == 0) __hip_atomic_store(misc_free, 1u, __ATOMIC_RELAXED, __HIP_MEMORY_SCOPE_WORKGROUP);
  WAIT_VM(0);
  late_tag(tag);
  if (lane == 0) flag_store((gu64*)(ws + OFF_FLAG) + FL_M + cg * 32 + rt, tag);
}

template <int NMT>
DEVI void conv_store(const f32x4 (&acc)[NMT], int mt0, int j, int d, float dsk, char* ws, LAS char* ub, LAS char* st, int lane) {
  const int g = lane >> 4, l15 = lane & 15;
#pragma unroll
  for (int m = 0; m < NMT; ++m)
    *(LAS u32x2*)(st + l15 * 128 + (mt0 + m) * 32 + g * 8) =
        u32x2{pk_bf16(acc[m][0], acc[m][1]), pk_bf16(acc[m][2], acc[m][3])};
  WAIT_LGKM0();
  const rsrc_t ra = mk_rsrc(ws + OFF_AT, SZ_AT);
  constexpr int PER = NMT * 2;
#pragma unroll
  for (int k = 0; k < 16 * PER / 64; ++k) {
    const int c = 64 * k + lane, blk = c / PER, pc = 2 * mt0 + c % PER;
    const u32x4 yv = *(LAS u32x4*)(st + blk * 128 + pc * 16);
    const u32x4 uv = *(LAS u32x4*)(ub + (1 + 16 * j + blk) * U_BLK + pc * 16);
    u32x4 o;
#pragma unroll
    for (int e = 0; e < 4; ++e) {
      const unsigned sk = pk_bf16(bf_lo(uv[e]) * dsk, bf_hi(uv[e]) * dsk);
      o[e] = pk_bf16(bf_lo(yv[e]) + bf_lo(sk), bf_hi(yv[e]) + bf_hi(sk));
    }
    bstore16_sc1(o, ra, ((16 * j + blk) * SD + d) * 128 + pc * 16);
  }
  WAIT_LGKM0();
}

struct TFrag { s16x4 lo, hi; };
DEVI s16x8 tf(const TFrag& f) { return cat4(f.lo, f.hi); }
DEVI unsigned ublk(unsigned ubg, int braw) { return (unsigned)(braw > (int)ubg ? braw : (int)ubg); }

struct C4Set { TFrag a[4]; s16x8 b[2]; };
#define C4_REGS(f) "+v"(f.a[0].lo), "+v"(f.a[0].hi), "+v"(f.a[1].lo), "+v"(f.a[1].hi), "+v"(f.a[2].lo), "+v"(f.a[2].hi), \
                   "+v"(f.a[3].lo), "+v"(f.a[3].hi), "+v"(f.b[0]), "+v"(f.b[1])
DEVI void c4_wait10(C4Set& f) { asm volatile("s_waitcnt lgkmcnt(10)" : C4_REGS(f) :: "memory"); }
DEVI void c4_wait0(C4Set& f) { asm volatile("s_waitcnt lgkmcnt(0)" : C4_REGS(f) :: "memory"); }
DEVI void c4_read(C4Set& n, unsigned aa, unsigned bb) {
  ASM_B128(n.b[0], bb, 0); ASM_B128(n.b[1], bb, 64);
  ASM_B64(n.a[0].lo, aa, 96); ASM_B64(n.a[0].hi, aa, 104); ASM_B64(n.a[1].lo, aa, 64); ASM_B64(n.a[1].hi, aa, 72);
  ASM_B64(n.a[2].lo, aa, 32); ASM_B64(n.a[2].hi, aa, 40); ASM_B64(n.a[3].lo, aa, 0); ASM_B64(n.a[3].hi, aa, 8);
}
template <bool RD>
DEVI void c4_step(f32x4 (&acc)[4], const C4Set& c, const TFrag& m2, const TFrag& m1, C4Set& n, unsigned aa, unsigned bb) {
#define C4(i, A, B, RDSTMT) acc[i] = mfma16(tf(A), B, acc[i]); SCHED_FENCE(); if (RD) { RDSTMT; } SCHED_FENCE();
  C4(0, m2, c.b[1], )
  C4(1, m1, c.b[1], ASM_B128(n.b[0], bb, 0); ASM_B128(n.b[1], bb, 64))
  C4(2, c.a[0], c.b[1], ASM_B64(n.a[0].lo, aa, 96); ASM_B64(n.a[0].hi, aa, 104))
  C4(3, c.a[1], c.b[1], ASM_B64(n.a[1].lo, aa, 64))
  C4(0, c.a[0], c.b[0], ASM_B64(n.a[1].hi, aa, 72))
  C4(1, c.a[1], c.b[0], ASM_B64(n.a[2].lo, aa, 32); ASM_B64(n.a[2].hi, aa, 40))
  C4(2, c.a[2], c.b[0], ASM_B64(n.a[3].lo, aa, 0))
  C4(3, c.a[3], c.b[0], ASM_B64(n.a[3].hi, aa, 8))
#undef C4
}
DEVI void conv_unit4(int j, int d, float dsk, char* ws, LAS char* cp, LAS char* ub, LAS char* st, int lane) {
  const int g = lane >> 4, r = lane & 15;
  const int i0 = 2047 - r + 8 * g, c = i0 & 3;
  unsigned aa = (unsigned)(uintptr_t)cp + c * CP_STRIDE + (i0 - c) * 2 - 96;
  const unsigned ubg = (unsigned)(uintptr_t)ub + 16 * g;
  int ai = (int)ubg + (1 + 16 * j + r) * U_BLK;
  f32x4 acc[4] = {f32x4{0, 0, 0, 0}, f32x4{0, 0, 0, 0}, f32x4{0, 0, 0, 0}, f32x4{0, 0, 0, 0}};
  C4Set f0, f1, f2;
  TFrag m2, m1;
  ASM_B64(m2.lo, aa, 160); ASM_B64(m2.hi, aa, 168); ASM_B64(m1.lo, aa, 128); ASM_B64(m1.hi, aa, 136);
  c4_read(f0, aa, ublk(ubg, ai));
  asm volatile("s_waitcnt lgkmcnt(10)" : "+v"(m2.lo), "+v"(m2.hi), "+v"(m1.lo), "+v"(m1.hi) :: "memory");
  aa -= 128; ai -= U_BLK;
  c4_read(f1, aa, ublk(ubg, ai));
  const int nd = 16 * (j + 1);
  const int n3 = nd / 3;
  for (int it = 0; it < n3; ++it) {
    c4_wait10(f0);
    aa -= 128; ai -= U_BLK;
    c4_step<true>(acc, f0, m2, m1, f2, aa, ublk(ubg, ai));
    c4_wait10(f1);
    aa -= 128; ai -= U_BLK;
    { const TFrag p2 = f0.a[2], p1 = f0.a[3]; c4_step<true>(acc, f1, p2, p1, f0, aa, ublk(ubg, ai)); }
    c4_wait10(f2);
    aa -= 128; ai -= U_BLK;
    { const TFrag p2 = f1.a[2], p1 = f1.a[3]; c4_step<true>(acc, f2, p2, p1, f1, aa, ublk(ubg, ai)); }
    m2 = f2.a[2]; m1 = f2.a[3];
  }
  c4_wait10(f0);
  c4_step<false>(acc, f0, m2, m1, f2, 0, 0);
  c4_wait0(f1);
  if (j) c4_step<false>(acc, f1, f0.a[2], f0.a[3], f2, 0, 0);
  conv_store<4>(acc, 0, j, d, dsk, ws, ub, st, lane);
}

struct C2Set { TFrag a[4]; s16x8 p[2], q[2]; };
#define C2_REGS(f) "+v"(f.a[0].lo), "+v"(f.a[0].hi), "+v"(f.a[1].lo), "+v"(f.a[1].hi), "+v"(f.a[2].lo), "+v"(f.a[2].hi), \
                   "+v"(f.a[3].lo), "+v"(f.a[3].hi), "+v"(f.p[0]), "+v"(f.p[1]), "+v"(f.q[0]), "+v"(f.q[1])
DEVI void c2_wait12(C2Set& f) { asm volatile("s_waitcnt lgkmcnt(12)" : C2_REGS(f) :: "memory"); }
DEVI void c2_wait10(C2Set& f) { asm volatile("s_waitcnt lgkmcnt(10)" : C2_REGS(f) :: "memory"); }
DEVI void c2_wait0(C2Set& f) { asm volatile("s_waitcnt lgkmcnt(0)" : C2_REGS(f) :: "memory"); }
template <bool RDQ>
DEVI void c2_read(C2Set& n, unsigned aa, unsigned bp, unsigned bq) {
  ASM_B128(n.p[0], bp, 0); ASM_B128(n.p[1], bp, 64);
  ASM_B64(n.a[0].lo, aa, 32); ASM_B64(n.a[0].hi, aa, 40); ASM_B64(n.a[1].lo, aa, 0); ASM_B64(n.a[1].hi, aa, 8);
  ASM_B64(n.a[2].lo, aa, 96); ASM_B64(n.a[2].hi, aa, 104); ASM_B64(n.a[3].lo, aa, 64); ASM_B64(n.a[3].hi, aa, 72);
  if (RDQ) { ASM_B128(n.q[0], bq, 0); ASM_B128(n.q[1], bq, 64); }
}
template <bool BOTH, bool RDQ, bool RD = true>
DEVI void c2_step(f32x4 (&c0)[2], f32x4 (&c1)[2], const C2Set& c, C2Set& n, unsigned aa, unsigned bp, unsigned bq) {
#define C2(ACC, A, B, RDSTMT) ACC = mfma16(tf(A), B, ACC); SCHED_FENCE(); if (RD) { RDSTMT; } SCHED_FENCE();
  C2(c1[0], c.a[0], c.p[0], ASM_B128(n.p[0], bp, 0); ASM_B128(n.p[1], bp, 64); ASM_B64(n.a[0].lo, aa, 32))
  C2(c1[1], c.a[1], c.p[0], ASM_B64(n.a[0].hi, aa, 40); ASM_B64(n.a[1].lo, aa, 0); ASM_B64(n.a[1].hi, aa, 8))
  C2(c1[0], c.a[2], c.p[1], ASM_B64(n.a[2].lo, aa, 96); ASM_B64(n.a[2].hi, aa, 104))
  C2(c1[1], c.a[3], c.p[1], ASM_B64(n.a[3].lo, aa, 64); ASM_B64(n.a[3].hi, aa, 72); if (RDQ && !BOTH) { ASM_B128(n.q[0], bq, 0); ASM_B128(n.q[1], bq, 64); })
  if (BOTH) {
    C2(c0[0], c.a[0], c.q[0], if (RDQ) { ASM_B128(n.q[0], bq, 0); })
    C2(c0[1], c.a[1], c.q[0], if (RDQ) { ASM_B128(n.q[1], bq, 64); })
    C2(c0[0], c.a[2], c.q[1], )
    C2(c0[1], c.a[3], c.q[1], )
  }
#undef C2
}
DEVI void conv_unit2(int mt0, int d, float dsk, char* ws, LAS char* cp, LAS char* ub, LAS char* st, int lane) {
  const int g = lane >> 4, r = lane & 15;
  const int i0 = 2047 - r + 8 * g, c = i0 & 3;
  unsigned aa = (unsigned)(uintptr_t)cp + c * CP_STRIDE + (i0 - c) * 2 - 32 * mt0 - 32;
  const unsigned ubg = (unsigned)(uintptr_t)ub + 16 * g;
  int ai = (int)ubg + (1 + r) * U_BLK;
  f32x4 c0[2] = {f32x4{0, 0, 0, 0}, f32x4{0, 0, 0, 0}}, c1[2] = {f32x4{0, 0, 0, 0}, f32x4{0, 0, 0, 0}};
  C2Set f0, f1, f2;
  c2_read<true>(f0, aa, ublk(ubg, ai + 16 * U_BLK), ublk(ubg, ai));
  aa -= 128; ai -= U_BLK;
  c2_read<true>(f1, aa, ublk(ubg, ai + 16 * U_BLK), ublk(ubg, ai));
  f2.q[0] = s16x8{0, 0, 0, 0, 0, 0, 0, 0}; f2.q[1] = f2.q[0];
  for (int it = 0; it < 6; ++it) {
    c2_wait12(f0);
    aa -= 128; ai -= U_BLK;
    c2_step<true, true>(c0, c1, f0, f2, aa, ublk(ubg, ai + 16 * U_BLK), ublk(ubg, ai));
    c2_wait12(f1);
    aa -= 128; ai -= U_BLK;
    c2_step<true, true>(c0, c1, f1, f0, aa, ublk(ubg, ai + 16 * U_BLK), ublk(ubg, ai));
    c2_wait12(f2);
    aa -= 128; ai -= U_BLK;
    c2_step<true, true>(c0, c1, f2, f1, aa, ublk(ubg, ai + 16 * U_BLK), ublk(ubg, ai));
  }
  c2_wait0(f0); c2_wait0(f1);
  for (int it = 0; it < 4; ++it) {
    c2_wait10(f0);
    aa -= 128; ai -= U_BLK;
    c2_step<false, false>(c0, c1, f0, f2, aa, ublk(ubg, ai + 16 * U_BLK), 0);
    c2_wait10(f1);
    aa -= 128; ai -= U_BLK;
    c2_step<false, false>(c0, c1, f1, f0, aa, ublk(ubg, ai + 16 * U_BLK), 0);
    c2_wait10(f2);
    aa -= 128; ai -= U_BLK;
    c2_step<false, false>(c0, c1, f2, f1, aa, ublk(ubg, ai + 16 * U_BLK), 0);
  }
  c2_wait10(f0);
  c2_step<false, false, false>(c0, c1, f0, f2, 0, 0, 0);
  c2_wait0(f1);
  c2_step<false, false, false>(c0, c1, f1, f2, 0, 0, 0);
  conv_store<2>(c0, mt0, 0, d, dsk, ws, ub, st, lane);
  conv_store<2>(c1, mt0, 1, d, dsk, ws, ub, st, lane);
}

DEVI void c_role(const u16* u, const u16* logA, const u16* Aim, const u16* Cre, const u16* Cim, const u16* logdt,
                 const u16* dskip, const u16* wg, const u16* wu, const u16* wd, const u16* gw, char* ws, LAS char* sm,
                 int i, int w, int lane, u64& tag) {
  const int xcd = i & 7, li = i >> 3;
  const int rt = 8 * (xcd & 3) + (li & 7), cg = 4 * (xcd >> 2) + (li >> 3);
  const int pi = rt >> 1, odd = rt & 1;
  const int dX = 80 * cg + 5 * pi + (odd ? 3 : 0), dY = dX + 1, dZ = 80 * cg + 5 * pi + 2;
  LAS unsigned* sync = (LAS unsigned*)(sm + C_SYNC);
  if (threadIdx.x < 4) sync[threadIdx.x] = 0u;
  WAIT_LGKM0();
  BARRIER();
  LAS char* st = sm + C_ST + w * 2048;
  if (w < 3) {
    const int d = w == 0 ? dX : w == 1 ? dY : dZ;
    LAS char* cp = sm + C_CP + w * CP_CH;
    LAS char* ub = sm + C_U + w * U_CH;
    const float dsk = bf2f(dskip[d]);
    kgen_wave(logA, Aim, Cre, Cim, logdt, cp, ub, sync + 3, d, lane, u, wg, wu, i, w, ws, tag);
    STAMP(1, 1);
    late_tag(tag);
    u_fill(ws, ub, d, cg, lane, tag);
    if (w < 2 && lane == 0) __hip_atomic_store(sync + w, 1u, __ATOMIC_RELAXED, __HIP_MEMORY_SCOPE_WORKGROUP);
    STAMP(1, 2);
    __builtin_amdgcn_s_setprio(PRIO_CONV);
    if (w < 2) conv_unit4(1, d, dsk, ws, cp, ub, st, lane);
    else conv_unit2(odd ? 2 : 0, d, dsk, ws, cp, ub, st, lane);
  } else {
    const float dx = bf2f(dskip[dX]), dy = bf2f(dskip[dY]);
    misc_wave(u, wg, wu, wd, gw, ws, sm + C_MISC, sync + 3, i, lane, tag);
    STAMP(1, 1);
    lds_wait(sync + 0, 1u);
    STAMP(1, 2);
    __builtin_amdgcn_s_setprio(PRIO_CONV);
    conv_unit4(0, dX, dx, ws, sm + C_CP, sm + C_U, st, lane);
    lds_wait(sync + 1, 1u);
    conv_unit4(0, dY, dy, ws, sm + C_CP + CP_CH, sm + C_U + U_CH, st, lane);
  }
  STAMP(1, 3);
  WAIT_VM(0);
  unsigned arrived = 0;
  if (lane == 0) arrived = __hip_atomic_fetch_add(sync + 2, 1u, __ATOMIC_RELAXED, __HIP_MEMORY_SCOPE_WORKGROUP);
  arrived = __builtin_amdgcn_readfirstlane(arrived);
  gu64* fl = (gu64*)(ws + OFF_FLAG);
  if (arrived == 3u) {
    if (lane == 0) flag_store(fl + FL_A + cg * 32 + rt, tag);
    STAMP(1, 4);
    if (i == 0) {
#pragma unroll 1
      for (unsigned spins = 0; spins < (1u << 18); ++spins) {
        u64 v[8];
#pragma unroll
        for (int k = 0; k < 4; ++k) { v[k] = flag_load(fl + FL_H + 64 * k + lane); v[4 + k] = flag_load(fl + FL_A + 64 * k + lane); }
        bool ok = true;
#pragma unroll
        for (int k = 0; k < 8; ++k) ok &= v[k] == tag;
        if (__all(ok)) break;
        __builtin_amdgcn_s_sleep(4);
      }
      if (lane == 0) flag_store(fl + FL_E, tag);
      STAMP(1, 5);
    }
  }
}

__global__ __launch_bounds__(256, 2) void gated_mix_fused(
    const u16* __restrict__ u, const u16* __restrict__ logA, const u16* __restrict__ Aim,
    const u16* __restrict__ Cre, const u16* __restrict__ Cim, const u16* __restrict__ logdt,
    const u16* __restrict__ dskip, const u16* __restrict__ wg, const u16* __restrict__ wu,
    const u16* __restrict__ wd, const u16* __restrict__ gw, char* __restrict__ ws, u16* __restrict__ out) {
  asm volatile("" :: "s"(u), "s"(logA), "s"(Aim), "s"(Cre), "s"(Cim), "s"(logdt), "s"(dskip), "s"(wg), "s"(wu), "s"(wd), "s"(gw), "s"(ws), "s"(out));
  extern __shared__ __attribute__((aligned(16))) char smem[];
  LAS char* sm = (LAS char*)smem;
  const int b = blockIdx.x, lane = threadIdx.x & 63;
  const int w = __builtin_amdgcn_readfirstlane(threadIdx.x >> 6);
  u64 tag = flag_load((gu64*)(ws + OFF_FLAG) + FL_E);
  if (b < 256) {
    STAMP(0, 0);
    __builtin_amdgcn_s_setprio(PRIO_G1);
    ffn_in(u, wg, wu, ws, sm, b, w, lane, tag);
    __builtin_amdgcn_s_setprio(PRIO_G2);
    ffn_out(out, ws, sm, b, w, lane, tag);
  } else {
    STAMP(1, 0);
    c_role(u, logA, Aim, Cre, Cim, logdt, dskip, wg, wu, wd, gw, ws, sm, b - 256, w, lane, tag);
  }
}

extern "C" void kernel_launch(void* const* d_in, const int* in_sizes, int n_in, void* d_out, int out_size,
                              void* d_ws, size_t ws_size, hipStream_t stream) {
  const u16* u = (const u16*)d_in[0];
  const u16* logA = (const u16*)d_in[1];
  const u16* Aim = (const u16*)d_in[2];
  const u16* Cre = (const u16*)d_in[3];
  const u16* Cim = (const u16*)d_in[4];
  const u16* logdt = (const u16*)d_in[5];
  const u16* dskip = (const u16*)d_in[6];
  const u16* wg = (const u16*)d_in[8];
  const u16* wu = (const u16*)d_in[9];
  const u16* wd = (const u16*)d_in[10];
  const u16* gw = (const u16*)d_in[11];
  (void)hipFuncSetAttribute((const void*)gated_mix_fused, hipFuncAttributeMaxDynamicSharedMemorySize, LDS_BYTES);
  gated_mix_fused<<<512, 256, LDS_BYTES, stream>>>(u, logA, Aim, Cre, Cim, logdt, dskip, wg, wu, wd, gw, (char*)d_ws, (u16*)d_out);
}
```
